```python
import torch

def _softmax(x, axis=-1):
    return torch.softmax(x, dim=axis)
def _gelu(x):
    return torch.nn.functional.gelu(x, approximate="tanh")
def _silu(x):
    return torch.nn.functional.silu(x)
def _sigmoid(x):
    return torch.sigmoid(x)
def _layernorm(x, axis=-1, eps=1e-5):
    m = x.mean(dim=axis, keepdim=True)
    v = x.var(dim=axis, keepdim=True, unbiased=False)
    return (x - m) / torch.sqrt(v + eps)
def _scatter_add(tgt, idx, src, axis=0):
    return tgt.clone().index_add_(axis, idx, src)
def _segment_sum(src, seg, n):
    out = torch.zeros((n,) + src.shape[1:], dtype=src.dtype)
    return out.index_add_(0, seg, src)
def _pad(x, pw):
    flat = tuple(v for pair in reversed(pw) for v in pair)
    return torch.nn.functional.pad(x, flat)

kernel_name = 'gated_mix__linear_attn_block__ssm_conv_s4d__prompt'

def setup_inputs():

    _w = (lambda a: (lambda t: t.to(torch.bfloat16) if t.is_floating_point() else t)(torch.as_tensor(a)))
    def _A_setup():
        B, S, D = 1, 2048, 576
        return {
            "x": _w((lambda *s: torch.randn(*s, dtype=torch.bfloat16) * 0.05)(B, S, D)),
            "wqkv": _w((lambda *s: torch.randn(*s, dtype=torch.bfloat16) * 0.8)(D, 3 * D)),
            "wo": _w((lambda *s: torch.randn(*s, dtype=torch.bfloat16) * 0.05)(D, D)),
        }
    _A_in = _A_setup()

    def _B_setup():
        Bsz, L, D, N = 1, 2048, 576, 128
        return {
            "u": _w((lambda *s: torch.randn(*s, dtype=torch.bfloat16) * 0.05)(Bsz, L, D)),
            "log_A_real": _w((lambda *s: torch.randn(*s, dtype=torch.bfloat16) * 6.4)(D, N) * 0.5),
            "A_imag": _w((lambda *s: torch.randn(*s, dtype=torch.bfloat16) * 0.05)(D, N) * 3.14),
            "C_real": _w((lambda *s: torch.randn(*s, dtype=torch.bfloat16) * 0.2)(D, N) / N ** 0.5),
            "C_imag": _w((lambda *s: torch.randn(*s, dtype=torch.bfloat16) * 0.2)(D, N) / N ** 0.5),
            "log_dt": _w((lambda *s: torch.randn(*s, dtype=torch.bfloat16) * 0.05)(D) * 0.5 - 4.0),
            "D_skip": _w((lambda *s: torch.randn(*s, dtype=torch.bfloat16) * 0.05)(D)),
        }
    _B_in = _B_setup()

    return {
        "A_x": _A_in["x"],
        "A_wqkv": _A_in["wqkv"],
        "A_wo": _A_in["wo"],
        "B_u": _B_in["u"],
        "B_log_A_real": _B_in["log_A_real"],
        "B_A_imag": _B_in["A_imag"],
        "B_C_real": _B_in["C_real"],
        "B_C_imag": _B_in["C_imag"],
        "B_log_dt": _B_in["log_dt"],
        "B_D_skip": _B_in["D_skip"],
        "gate_w": _w((lambda *s: torch.randn(*s, dtype=torch.bfloat16) * 0.05)(576) * 16.0),
    }

def reference(A_x, A_wqkv, A_wo, B_u, B_log_A_real, B_A_imag, B_C_real, B_C_imag, B_log_dt, B_D_skip, gate_w):
    def _A_ref(x, wqkv, wo):
        B, S, D = x.shape
        H = 32; Dh = D // H
        qkv = (lambda x, *d: x.permute(*d))((x @ wqkv).reshape(B, S, 3, H, Dh), 2, 0, 3, 1, 4)
        q, k, v = qkv[0], qkv[1], qkv[2]
        phi = lambda t: torch.where(t > 0, t + 1, torch.exp(t)) + 1e-6
        pq, pk = phi(q), phi(k)
        kv = torch.einsum("bhsd,bhse->bhde", pk, v)
        z = torch.einsum("bhsd->bhd", pk)
        num = torch.einsum("bhqd,bhde->bhqe", pq, kv)
        den = torch.einsum("bhqd,bhd->bhq", pq, z)[..., None] + 1e-6
        o = num / den
        return (lambda x, *d: x.permute(*d))(o, 0, 2, 1, 3).reshape(B, S, D) @ wo

    def _B_ref(u, log_A_real, A_imag, C_real, C_imag, log_dt, D_skip):
        L = u.shape[1]
        dt = torch.exp(log_dt)
        A = -torch.exp(log_A_real) + 1j * A_imag
        C = (C_real + 1j * C_imag) * dt[:, None]
        pos = (lambda x, dt: x.to(dt))(torch.arange(L), log_dt.dtype)
        P = torch.exp((A * dt[:, None])[:, :, None] * pos[None, None, :])
        K = 2.0 * torch.einsum("dn,dnl->dl", C, P).real
        L2 = 2 * L
        u_t = (lambda x, *d: x.permute(*d))(u, 0, 2, 1)
        Kf = (lambda x, *a, **k: torch.fft.rfft(x.float(), *a, **k))(K, L2)
        Uf = (lambda x, *a, **k: torch.fft.rfft(x.float(), *a, **k))(u_t, L2)
        y = (lambda x, *a, **k: torch.fft.irfft(x, *a, **k).to(torch.bfloat16))(Uf * Kf[None, :, :], L2)[:, :, :L]
        y = (lambda x, *d: x.permute(*d))(y, 0, 2, 1)
        return y + u * D_skip[None, None, :]

    x = A_x
    g = _sigmoid((x * gate_w).sum(-1, keepdims=True))
    a = _A_ref(x, A_wqkv, A_wo)
    b = _B_ref(x, B_log_A_real, B_A_imag, B_C_real, B_C_imag, B_log_dt, B_D_skip)
    return g * a + (1 - g) * b
```

```cpp
#include <hip/hip_runtime.h>
#include <hip/hip_bf16.h>
#include <stdint.h>

typedef unsigned short u16;
static __device__ __forceinline__ float bf2f(u16 v){ return __uint_as_float(((unsigned)v)<<16); }
static __device__ __forceinline__ u16 f2bf(float f){ unsigned u=__float_as_uint(f); u += 0x7fffu + ((u>>16)&1u); return (u16)(u>>16); }
static __device__ __forceinline__ float rbf(float f){ return bf2f(f2bf(f)); }

constexpr int S=2048, D=576, H=32, DH=18, N=128;

__global__ void k_qkv(const u16* x, const u16* w, u16* qkv){
  int col = blockIdx.x*64+threadIdx.x; int row = blockIdx.y;
  float acc=0.f;
  for(int k=0;k<D;++k) acc += bf2f(x[row*D+k])*bf2f(w[k*3*D+col]);
  float t = rbf(acc);
  if(col < 2*D){ float p = t>0.f ? rbf(t+1.f) : rbf(expf(t)); t = rbf(p+1e-6f); }
  qkv[row*3*D+col]=f2bf(t);
}
__global__ void k_kv(const u16* qkv, float* kv, float* z){
  int h=blockIdx.x; int t=threadIdx.x;
  if(t>=DH*(DH+1)) return;
  int d=t/(DH+1), e=t%(DH+1);
  float acc=0.f;
  for(int s=0;s<S;++s){
    float pk=bf2f(qkv[s*3*D+D+h*DH+d]);
    float v = e<DH ? bf2f(qkv[s*3*D+2*D+h*DH+e]) : 1.f;
    acc += pk*v;
  }
  if(e<DH) kv[(h*DH+d)*DH+e]=rbf(acc); else z[h*DH+d]=rbf(acc);
}
__global__ void k_o(const u16* qkv, const float* kv, const float* z, u16* o){
  int c = blockIdx.x*64+threadIdx.x; int row=blockIdx.y;
  int h=c/DH, e=c%DH;
  float num=0.f, den=0.f;
  for(int d=0;d<DH;++d){ float pq=bf2f(qkv[row*3*D+h*DH+d]); num+=pq*kv[(h*DH+d)*DH+e]; den+=pq*z[h*DH+d]; }
  num=rbf(num); den=rbf(rbf(den)+1e-6f);
  o[row*D+c]=f2bf(num/den);
}
__global__ void k_K(const u16* logA, const u16* Aim, const u16* Cr, const u16* Ci, const u16* logdt, float* K){
  int l = blockIdx.x*256+threadIdx.x; int d=blockIdx.y;
  float dt = rbf(expf(bf2f(logdt[d])));
  float pos = rbf((float)l);
  float acc=0.f;
  for(int n=0;n<N;++n){
    float ar = -rbf(expf(bf2f(logA[d*N+n])))*dt;
    float ai = bf2f(Aim[d*N+n])*dt;
    float cr = bf2f(Cr[d*N+n])*dt, ci=bf2f(Ci[d*N+n])*dt;
    float e = expf(ar*pos); float s,c; sincosf(ai*pos,&s,&c);
    acc += cr*(e*c) - ci*(e*s);
  }
  K[d*S+l]=2.f*acc;
}
__global__ void k_fin(const u16* x, const u16* o, const u16* wo, const float* K, const u16* Dsk, const u16* gw, u16* out){
  int c = blockIdx.x*64+threadIdx.x; int row=blockIdx.y;
  float gs=0.f;
  for(int k=0;k<D;++k) gs += rbf(bf2f(x[row*D+k])*bf2f(gw[k]));
  gs=rbf(gs);
  float g = rbf(1.f/(1.f+expf(-gs)));
  float a=0.f;
  for(int k=0;k<D;++k) a += bf2f(o[row*D+k])*bf2f(wo[k*D+c]);
  a=rbf(a);
  float y=0.f;
  for(int j=0;j<=row;++j) y += K[c*S+j]*bf2f(x[(row-j)*D+c]);
  y=rbf(y);
  float b = rbf(y + rbf(bf2f(x[row*D+c])*bf2f(Dsk[c])));
  float r = rbf(g*a) + rbf(rbf(1.f-g)*b);
  out[row*D+c]=f2bf(r);
}

extern "C" void kernel_launch(void* const* d_in, const int* in_sizes, int n_in,
                              void* d_out, int out_size, void* d_ws, size_t ws_size,
                              hipStream_t stream) {
  const u16* x=(const u16*)d_in[0]; const u16* wqkv=(const u16*)d_in[1]; const u16* wo=(const u16*)d_in[2];
  const u16* logA=(const u16*)d_in[4]; const u16* Aim=(const u16*)d_in[5]; const u16* Cr=(const u16*)d_in[6];
  const u16* Ci=(const u16*)d_in[7]; const u16* logdt=(const u16*)d_in[8]; const u16* Dsk=(const u16*)d_in[9];
  const u16* gw=(const u16*)d_in[10];
  char* ws=(char*)d_ws;
  u16* qkv=(u16*)ws; ws+= (size_t)S*3*D*2;
  u16* o=(u16*)ws; ws+=(size_t)S*D*2;
  float* kv=(float*)ws; ws+=H*DH*DH*4;
  float* z=(float*)ws; ws+=H*DH*4+256;
  float* K=(float*)(((uintptr_t)ws+255)&~(uintptr_t)255);
  k_qkv<<<dim3(3*D/64,S),64,0,stream>>>(x,wqkv,qkv);
  k_kv<<<H,384,0,stream>>>(qkv,kv,z);
  k_o<<<dim3(D/64,S),64,0,stream>>>(qkv,kv,z,o);
  k_K<<<dim3(S/256,D),256,0,stream>>>(logA,Aim,Cr,Ci,logdt,K);
  k_fin<<<dim3(D/64,S),64,0,stream>>>(x,o,wo,K,Dsk,gw,(u16*)d_out);
}
```

```cpp
#include <hip/hip_runtime.h>
#include <stdint.h>

typedef unsigned int u32;
typedef unsigned short u16;
typedef unsigned long long u64;
typedef __attribute__((ext_vector_type(2))) unsigned u32x2;
typedef __attribute__((ext_vector_type(4))) unsigned u32x4;
typedef __attribute__((ext_vector_type(2))) float f32x2;
typedef __attribute__((ext_vector_type(4))) float f32x4;
typedef __attribute__((ext_vector_type(4))) short s16x4;
typedef __attribute__((ext_vector_type(2))) __bf16 bf16x2;
typedef __attribute__((ext_vector_type(8))) __bf16 bf16x8;
#define LAS __attribute__((address_space(3)))
#define GAS __attribute__((address_space(1)))
#define DI __device__ __forceinline__

constexpr int SEQ = 2048, DM = 576;
constexpr int XROW = DM * 2;
constexpr int WROW = 3 * DM * 2;
constexpr int WS_PQ = 0;
constexpr int WS_M = WS_PQ + 128 * 18 * 1024;
constexpr int WS_BT = WS_M + 36 * 18 * 1024;
constexpr int WS_G = WS_BT + DM * SEQ * 2;
constexpr int WS_MBKV = WS_G + SEQ * 4;
constexpr int MBKV_SZ = 2 * 16 * 18 * 16;
constexpr int WS_MBZ = WS_MBKV + 128 * MBKV_SZ;
constexpr int MBZ_SZ = 2 * 16 * 20 * 16;
constexpr int WS_EP = WS_MBZ + 44 * MBZ_SZ;
constexpr int WS_END = WS_EP + 1024;
constexpr int K1_LDS = 4 * 34816 + 2048;
constexpr int K2_LDS = 5 * 18 * 1024 + 80 * 128;

DI float bf2f(u32 v) { return __uint_as_float(v << 16); }
DI float bfhi(u32 v) { return __uint_as_float(v & 0xffff0000u); }
DI u32 pk(float a, float b) { bf16x2 v = {(__bf16)a, (__bf16)b}; return __builtin_bit_cast(u32, v); }
DI float rbf(float a) { return bf2f(pk(a, a) & 0xffffu); }
DI f32x4 mfma(u32x4 a, u32x4 b, f32x4 c) {
  return __builtin_amdgcn_mfma_f32_16x16x32_bf16(__builtin_bit_cast(bf16x8, a), __builtin_bit_cast(bf16x8, b), c, 0, 0, 0);
}
DI u32x4 ldsr128(u32 a) { return *(LAS u32x4*)(size_t)a; }
DI u32x2 ldsr64(u32 a) { return *(LAS u32x2*)(size_t)a; }
DI u32 ldsr32(u32 a) { return *(LAS u32*)(size_t)a; }
DI void ldsw128(u32 a, u32x4 v) { *(LAS u32x4*)(size_t)a = v; }
DI void ldsw64(u32 a, u32x2 v) { *(LAS u32x2*)(size_t)a = v; }
DI void ldsw32(u32 a, u32 v) { *(LAS u32*)(size_t)a = v; }
DI void ldsw16(u32 a, u32 v) { *(LAS u16*)(size_t)a = (u16)v; }
DI u32x2 ldstr(u32 a) { return __builtin_bit_cast(u32x2, __builtin_amdgcn_ds_read_tr16_b64_v4i16((LAS s16x4*)(size_t)a)); }
DI u32x4 ldstr2(u32 a0, u32 a1) { u32x2 l = ldstr(a0), h = ldstr(a1); return u32x4{l[0], l[1], h[0], h[1]}; }
DI void glds(const void* sbase, u32 voff, u32 lds) {
  asm volatile("s_mov_b32 m0, %2\n\ts_nop 0\n\tglobal_load_lds_dwordx4 %0, %1" ::"v"(voff), "s"(sbase), "s"(lds) : "memory");
}
#define WAITV(n) asm volatile("s_waitcnt vmcnt(" #n ")" ::: "memory")
#define WAITL0() asm volatile("s_waitcnt lgkmcnt(0)" ::: "memory")
#define BAR() do { asm volatile("" ::: "memory"); __builtin_amdgcn_s_barrier(); asm volatile("" ::: "memory"); } while (0)
#define CFENCE() asm volatile("" ::: "memory")
#define SCHED() __builtin_amdgcn_sched_barrier(0)
template <int N> DI void waitv() {
  if constexpr (N == 0) WAITV(0); else if constexpr (N == 5) WAITV(5); else if constexpr (N == 6) WAITV(6);
  else if constexpr (N == 10) WAITV(10); else if constexpr (N == 12) WAITV(12); else if constexpr (N == 15) WAITV(15);
  else static_assert(N < 0, "waitv");
}
DI float phi(float t) {
  float e = __builtin_amdgcn_exp2f(fminf(t, 0.f) * 1.44269504089f);
  return (fmaxf(t, 0.f) + 1e-6f) + e;
}
DI float rowsum16(float v) {
  v += __builtin_bit_cast(float, __builtin_amdgcn_update_dpp(0, __builtin_bit_cast(int, v), 0x128, 0xf, 0xf, false));
  v += __builtin_bit_cast(float, __builtin_amdgcn_update_dpp(0, __builtin_bit_cast(int, v), 0x124, 0xf, 0xf, false));
  v += __builtin_bit_cast(float, __builtin_amdgcn_update_dpp(0, __builtin_bit_cast(int, v), 0x122, 0xf, 0xf, false));
  v += __builtin_bit_cast(float, __builtin_amdgcn_update_dpp(0, __builtin_bit_cast(int, v), 0x121, 0xf, 0xf, false));
  return v;
}
typedef __amdgpu_buffer_rsrc_t rsrc_t;
DI void st_sc1(rsrc_t r, u32 off, u32x4 v) { __builtin_amdgcn_raw_buffer_store_b128(v, r, off, 0, 16); }
DI u32x4 ld_sc1(rsrc_t r, u32 off) { return __builtin_amdgcn_raw_buffer_load_b128(r, off, 0, 16); }
DI u32 ld_ep(char* ws, int i) { return __hip_atomic_load((GAS u32*)(ws + WS_EP) + i, __ATOMIC_RELAXED, __HIP_MEMORY_SCOPE_AGENT); }
DI void st_ep(char* ws, int i, u32 v) { __hip_atomic_store((GAS u32*)(ws + WS_EP) + i, v, __ATOMIC_RELAXED, __HIP_MEMORY_SCOPE_AGENT); }
constexpr u32 SPIN_CAP = 1u << 15;

template <int ROLE>
DI void gemm_main(const char* x, const char* w, const char* gw, int row0, int cg, int c0, int wave, int lane,
                  f32x4 (&acc)[ROLE ? 3 : 2][5]) {
  constexpr int RT = ROLE ? 3 : 2, NS = ROLE ? 3 : 4, XB = RT * 8192, STG = XB + 18432, P = RT + 3;
  constexpr int GWL = 4 * 34816;
  const int rg = wave & 3, ch = wave >> 2, fr = lane & 15, fh = lane >> 4;
  u32 xo[RT], wo[3];
#pragma unroll
  for (int i = 0; i < RT; ++i) {
    int row = 8 * (wave * RT + i) + (lane >> 3);
    int gr = row0 + row; gr = gr > SEQ - 1 ? SEQ - 1 : gr;
    xo[i] = gr * XROW + (((lane & 7) ^ ((row >> 1) & 7)) << 4);
  }
#pragma unroll
  for (int i = 0; i < 3; ++i) {
    int f = 64 * i + lane; f = f > 143 ? 143 : f;
    int lr = f / 18, cc = f - lr * 18;
    int kr = 8 * wave + (lr ^ ((wave & 1) << 2));
    int cb = ROLE ? 288 * cg + 16 * cc : (cc < 9 ? 1152 + 144 * cg + 16 * cc : 2304 - 144 + 144 * cg + 16 * cc);
    wo[i] = kr * WROW + cb;
  }
  auto issue = [&](int cidx, int slot) {
    const char* xb = x + cidx * 128;
    const char* wb = w + cidx * (64 * WROW);
    u32 sb = slot * STG;
#pragma unroll
    for (int i = 0; i < RT; ++i) glds(xb, xo[i], sb + (wave * RT + i) * 1024);
    glds(wb, wo[0], sb + XB + wave * 2304);
    glds(wb, wo[1], sb + XB + wave * 2304 + 1024);
    if (lane < 16) glds(wb, wo[2], sb + XB + wave * 2304 + 2048);
  };
  if (ROLE == 0 && wave < 2) { if (wave * 64 + lane < 72) glds(gw, (wave * 64 + lane) * 16, GWL + wave * 1024); }
  {
    int c = c0;
#pragma unroll
    for (int s = 0; s < NS; ++s) { issue(c, s); c = c == 8 ? 0 : c + 1; }
  }
  const int q4 = (lane >> 2) & 3, p4 = lane & 3;
  const u32 ax = (rg * RT * 16 + fr) * 128 + ((fh ^ ((fr >> 1) & 7)) << 4);
  const u32 r0 = (8 * fh + q4 + 4 * (fh & 1)) * 288 + XB, r1 = (8 * fh + q4 + 4 * ((fh & 1) ^ 1)) * 288 + XB;
  const u32 cm = ROLE ? 72 * p4 + 40 * ch : 72 * ch + 8 * p4;
  const u32 cx = ROLE ? 72 * p4 + 32 : 72 * ch + 32 + 144 * (p4 & 1);
  auto frags = [&](u32 sb, int cidx, int s, u32x4 (&a)[RT], u32x4 (&b)[5]) {
#pragma unroll
    for (int i = 0; i < RT; ++i) a[i] = ldsr128(sb + ((ax + i * 2048) ^ (s * 64)));
#pragma unroll
    for (int t = 0; t < 4; ++t) {
      const int to = (ROLE ? 8 * t : 40 * (t & 1) + 144 * (t >> 1)) + s * 9216;
      b[t] = ldstr2(sb + r0 + cm + to, sb + r1 + cm + to);
    }
    b[4] = ldstr2(sb + r0 + cx + s * 9216, sb + r1 + cx + s * 9216);
    if (ROLE == 0) {
      u32x4 g = ldsr128(GWL + cidx * 128 + s * 64 + fh * 16);
      if (fr >= 8) b[4] = g;
    }
  };
  auto mma = [&](u32x4 (&a)[RT], u32x4 (&b)[5]) {
#pragma unroll
    for (int t = 0; t < 5; ++t)
#pragma unroll
      for (int i = 0; i < RT; ++i) acc[i][t] = ROLE ? mfma(b[t], a[i], acc[i][t]) : mfma(a[i], b[t], acc[i][t]);
  };
#pragma unroll
  for (int i = 0; i < RT; ++i)
#pragma unroll
    for (int t = 0; t < 5; ++t) acc[i][t] = f32x4{0.f, 0.f, 0.f, 0.f};
  u32x4 a0[RT], b0[5], a1[RT], b1[5];
  waitv<(NS - 1) * P>();
  BAR();
  int cc = c0, cn = c0 + NS; cn = cn >= 9 ? cn - 9 : cn;
  int slot = 0;
  frags(0, cc, 0, a0, b0);
#pragma unroll 1
  for (int it = 0; it < 9; ++it) {
    frags(slot * STG, cc, 1, a1, b1);
    mma(a0, b0);
    if (it < 8) {
      if (it <= 9 - NS) waitv<(NS - 2) * P>();
      else if (NS == 4 && it == 6) waitv<P>();
      else waitv<0>();
      WAITL0();
      BAR();
      if (it + NS < 9) issue(cn, slot);
      cn = cn == 8 ? 0 : cn + 1;
      cc = cc == 8 ? 0 : cc + 1;
      slot = slot == NS - 1 ? 0 : slot + 1;
      frags(slot * STG, cc, 0, a0, b0);
    }
    mma(a1, b1);
  }
}

DI void role_kv(const char* x, const char* w, const char* wo_g, const char* gw, char* ws, int rb, int g, int c0, int wave, int lane) {
  f32x4 acc[2][5];
  gemm_main<0>(x, w, gw, rb * 128, g, c0, wave, lane, acc);
  constexpr u32 XCH = 34816, WOI = XCH + 40960, KVA = WOI + 21504;
  const int fr = lane & 15, fh = lane >> 4, rg = wave & 3, hp = wave >> 2;
  const int chead = 4 * g + (rb & 3), chq = rb >> 2;
  {
    const char* src = wo_g + chead * (18 * XROW);
#pragma unroll
    for (int i = 0; i < 3; ++i) {
      int pc = wave + 8 * i, f = pc * 64 + lane;
      if (pc <= 20) { if (f < 1296) glds(src, f * 16, WOI + pc * 1024); }
    }
  }
  const int hi = wave & 3, part = wave >> 2, odd = hi & 1;
  const int jq = fr < 4 ? fr : fr - 4;
  const bool isz = (fr < 4 || fr >= 8);
  const int ckv = g * 16 + hi + (part ? 0 : 4 * fh);
  const int cz = (g >> 1) * 11 + (jq > 10 ? 10 : jq);
  const u32 tkv = ld_ep(ws, ckv) + 1, tz = ld_ep(ws, 128 + cz) + 1, tme = ld_ep(ws, g * 16 + rb) + 1;
  if (wave == 0) ldsw128(KVA + lane * 16, u32x4{0, 0, 0, 0});
  if (g == 0 && hp == 0 && fr == 8) {
#pragma unroll
    for (int i = 0; i < 2; ++i) {
      f32x4 gv;
#pragma unroll
      for (int j = 0; j < 4; ++j) gv[j] = rbf(1.f / (1.f + __expf(-rbf(acc[i][4][j]))));
      *(f32x4*)(ws + WS_G + (rb * 128 + rg * 32 + i * 16 + fh * 4) * 4) = gv;
    }
  }
#pragma unroll
  for (int t = 0; t < 5; ++t) {
    float v[8];
#pragma unroll
    for (int i = 0; i < 2; ++i)
#pragma unroll
      for (int j = 0; j < 4; ++j) {
        float a = acc[i][t][j];
        v[i * 4 + j] = t < 2 ? phi(a) : (t == 4 ? (fr < 4 ? phi(a) : a) : a);
      }
    ldsw128(XCH + ((hp * 4 + rg) * 5 + t) * 1024 + lane * 16, u32x4{pk(v[0], v[1]), pk(v[2], v[3]), pk(v[4], v[5]), pk(v[6], v[7])});
  }
  WAITL0();
  BAR();
  rsrc_t rs = __builtin_amdgcn_make_buffer_rsrc(ws, 0, WS_END, 0x00020000);
  {
    f32x4 t1 = {0.f, 0.f, 0.f, 0.f}, t2 = {0.f, 0.f, 0.f, 0.f};
    const u32 base = XCH + (hi >> 1) * 20480 + lane * 16;
#pragma unroll
    for (int r = 0; r < 4; ++r) {
      u32x4 xf = ldsr128(base + r * 5120 + 4096);
      u32x4 vf = ldsr128(base + r * 5120 + 2048 + odd * 1024);
      u32x4 kf = ldsr128(base + r * 5120 + (part ? 4096 : odd * 1024));
      if (isz) xf = u32x4{0x3f803f80u, 0x3f803f80u, 0x3f803f80u, 0x3f803f80u};
      t1 = mfma(kf, vf, t1);
      t2 = mfma(kf, xf, t2);
    }
    u32 a0 = pk(t1[0], t1[1]), a1 = pk(t1[2], t1[3]), b0 = pk(t2[0], t2[1]), b1 = pk(t2[2], t2[3]);
    if (part) { a0 = odd ? a1 : a0; b0 = odd ? b1 : b0; }
    const u32 mkv = WS_MBKV + ckv * MBKV_SZ + part * (MBKV_SZ / 2) + rb * 288;
    const u32 mz = WS_MBZ + cz * MBZ_SZ + (g & 1) * (MBZ_SZ / 2) + rb * 320 + (hi * 5 + (part ? 4 : fh)) * 16;
    if (!part || fh == 0) {
      st_sc1(rs, mkv + fr * 16, u32x4{a0, tkv, a1, tkv});
      const bool mine = isz ? jq < 11 : ((fr >> 1) & 1) == odd;
      const u32 tg = isz ? tz : tkv;
      if (mine) st_sc1(rs, isz ? mz : mkv + (16 + (fr & 1)) * 16, u32x4{b0, tg, b1, tg});
    }
  }
  WAITV(0);
  BAR();
  u32x4 wf[5];
  {
    const int codd = chead & 1, q4 = (lane >> 2) & 3, p4 = lane & 3;
    int s0 = 8 * fh + q4, s1 = s0 + 4;
    s0 = s0 > 17 ? 17 : s0; s1 = s1 > 17 ? 17 : s1;
    s0 += 2 * codd; s1 += 2 * codd;
    s0 = s0 >= 18 ? s0 - 18 : s0; s1 = s1 >= 18 ? s1 - 18 : s1;
#pragma unroll
    for (int i = 0; i < 5; ++i) {
      int ct = wave + 8 * i; ct = ct > 35 ? 35 : ct;
      wf[i] = ldstr2(WOI + s0 * XROW + ct * 32 + p4 * 8, WOI + s1 * XROW + ct * 32 + p4 * 8);
    }
  }
  if (wave < 5) {
    int sg = 4 * wave + fh; const bool ok0 = sg < 18; sg = ok0 ? sg : 17;
    const u32 off = WS_MBKV + (g * 16 + rb) * MBKV_SZ + fr * 288 + sg * 16;
    u32x4 m, e;
    for (u32 spin = 0;; ++spin) {
      m = ld_sc1(rs, off);
      e = chq == 0 ? ld_sc1(rs, off + MBKV_SZ / 2) : m;
      bool ok = m[1] == tme && m[3] == tme && e[1] == tme;
      if (__all(ok) || spin > SPIN_CAP) break;
      __builtin_amdgcn_s_sleep(1);
    }
    float s[6];
    s[0] = rowsum16(bf2f(m[0])); s[1] = rowsum16(bfhi(m[0])); s[2] = rowsum16(bf2f(m[2])); s[3] = rowsum16(bfhi(m[2]));
    s[4] = rowsum16(bf2f(e[0])); s[5] = rowsum16(bfhi(e[0]));
    float sv = fr == 0 ? s[0] : fr == 1 ? s[1] : fr == 2 ? s[2] : fr == 3 ? s[3] : fr == 4 ? s[4] : s[5];
    if (ok0 && fr < 6) ldsw16(KVA + fr * 64 + sg * 2, pk(sv, sv));
  }
  WAITL0();
  BAR();
  if (wave == 0 && lane == 0) st_ep(ws, g * 16 + rb, tme);
  {
    u32x4 af = ldsr128(KVA + fr * 64 + fh * 16);
    const int un = 2 * (chead >> 3) + (chead & 1), hh = (chead >> 1) & 3;
#pragma unroll
    for (int i = 0; i < 5; ++i) {
      const int ct = wave + 8 * i;
      f32x4 c = mfma(af, wf[i], f32x4{0.f, 0.f, 0.f, 0.f});
      if (ct < 36) {
        const int cb0 = ct < 20 ? (ct / 5) * 5 : (ct & ~3);
        const int o = (ct - cb0) * 16 + fr;
        const int t = o < 64 ? (o & 3) : 4, cl = o < 64 ? (o >> 2) : o - 64;
        char* dst = ws + WS_M + (cb0 + t) * (18 * 1024) + (hh * 16 + cl) * 16;
        if (fh == 0) *(u32x2*)(dst + (2 * un + (chq >> 1)) * 1024 + (chq & 1) * 8) = u32x2{pk(c[0], c[1]), pk(c[2], c[3])};
        if (fh == 1 && chq == 0) *(u32*)(dst + (16 + (un >> 2)) * 1024 + (un & 3) * 4) = pk(c[0], c[1]);
      }
    }
  }
}

DI void role_q(const char* x, const char* w, char* ws, int jq, int hg, int c0, int wave, int lane) {
  f32x4 acc[3][5];
  gemm_main<1>(x, w, nullptr, jq * 192, hg, c0, wave, lane, acc);
  constexpr u32 ZL = 4 * 34816;
  const int fr = lane & 15, fh = lane >> 4, rg = wave & 3, par = wave >> 2;
  const int me = hg * 11 + jq;
  const u32 tme = ld_ep(ws, 128 + me) + 1;
#pragma unroll
  for (int i = 0; i < 3; ++i) {
#pragma unroll
    for (int t = 0; t < 4; ++t)
#pragma unroll
      for (int j = 0; j < 4; ++j) acc[i][t][j] = phi(acc[i][t][j]);
    float e0 = par ? acc[i][4][2] : acc[i][4][0], e1 = par ? acc[i][4][3] : acc[i][4][1];
    acc[i][4][0] = phi(e0); acc[i][4][1] = phi(e1);
  }
  rsrc_t rs = __builtin_amdgcn_make_buffer_rsrc(ws, 0, WS_END, 0x00020000);
  {
    const int i0 = wave * 4 + fh, i1 = (wave < 2 ? 32 : 0) + i0;
    const u32 base = WS_MBZ + me * MBZ_SZ + fr * 320;
    const u32 o0 = base + (i0 >= 20 ? MBZ_SZ / 2 + (i0 - 20) * 16 : i0 * 16);
    const u32 o1 = base + (i1 >= 20 ? MBZ_SZ / 2 + (i1 - 20) * 16 : i1 * 16);
    u32x4 m, e;
    for (u32 spin = 0;; ++spin) {
      m = ld_sc1(rs, o0);
      e = wave < 2 ? ld_sc1(rs, o1) : m;
      bool ok = m[1] == tme && m[3] == tme && e[1] == tme && e[3] == tme;
      if (__all(ok) || spin > SPIN_CAP) break;
      __builtin_amdgcn_s_sleep(1);
    }
    f32x4 s0 = {rowsum16(bf2f(m[0])), rowsum16(bfhi(m[0])), rowsum16(bf2f(m[2])), rowsum16(bfhi(m[2]))};
    if (fr == 0) ldsw128(ZL + i0 * 16, __builtin_bit_cast(u32x4, s0));
    if (wave < 2) {
      f32x4 s1 = {rowsum16(bf2f(e[0])), rowsum16(bfhi(e[0])), rowsum16(bf2f(e[2])), rowsum16(bfhi(e[2]))};
      if (fr == 0) ldsw128(ZL + i1 * 16, __builtin_bit_cast(u32x4, s1));
    }
  }
  WAITL0();
  BAR();
  if (wave == 0 && lane == 0) st_ep(ws, 128 + me, tme);
  f32x4 z[5];
#pragma unroll
  for (int t = 0; t < 5; ++t) z[t] = __builtin_bit_cast(f32x4, ldsr128(ZL + (2 * fh + par) * 80 + t * 16));
  const int un = 2 * hg + par;
#pragma unroll
  for (int i = 0; i < 3; ++i) {
    float d = acc[i][4][0] * z[4][0] + acc[i][4][1] * z[4][1];
#pragma unroll
    for (int t = 0; t < 4; ++t)
#pragma unroll
      for (int j = 0; j < 4; ++j) d += acc[i][t][j] * z[t][j];
    const float r = __builtin_amdgcn_rcpf(d + 1e-6f);
    const int rt = jq * 12 + rg * 3 + i;
    if (rt < 128) {
      char* dst = ws + WS_PQ + rt * (18 * 1024) + lane * 16;
#pragma unroll
      for (int s = 0; s < 2; ++s)
        *(u32x4*)(dst + (2 * un + s) * 1024) =
            u32x4{pk(acc[i][2 * s][0] * r, acc[i][2 * s][1] * r), pk(acc[i][2 * s][2] * r, acc[i][2 * s][3] * r),
                  pk(acc[i][2 * s + 1][0] * r, acc[i][2 * s + 1][1] * r), pk(acc[i][2 * s + 1][2] * r, acc[i][2 * s + 1][3] * r)};
      *(u32*)(dst + (16 + (un >> 2)) * 1024 + (un & 3) * 4) = pk(acc[i][4][0] * r, acc[i][4][1] * r);
    }
  }
}

struct cx2 { f32x2 r, i; };
DI cx2 cmul(cx2 a, cx2 b) { return cx2{a.r * b.r - a.i * b.i, a.r * b.i + a.i * b.r}; }
DI cx2 cpow(f32x2 lr, f32x2 rv, float n) {
  cx2 o;
#pragma unroll
  for (int m = 0; m < 2; ++m) {
    float mag = __builtin_amdgcn_exp2f(lr[m] * n), ph = __builtin_amdgcn_fractf(rv[m] * n);
    o.r[m] = mag * __builtin_amdgcn_cosf(ph);
    o.i[m] = mag * __builtin_amdgcn_sinf(ph);
  }
  return o;
}
DI u32x4 boxfrag(int xb, int lo, int hi) {
  u32x4 o;
#pragma unroll
  for (int d = 0; d < 4; ++d) {
    u32 a = (u32)(xb - 2 * d - lo) <= (u32)(hi - lo) ? 0x3f80u : 0u;
    u32 b = (u32)(xb - 2 * d - 1 - lo) <= (u32)(hi - lo) ? 0x3f800000u : 0u;
    o[d] = a | b;
  }
  return o;
}
DI u32x4 lds4x32(u32 a) { return u32x4{ldsr32(a), ldsr32(a + 4), ldsr32(a + 8), ldsr32(a + 12)}; }
DI u32x4 pack2(f32x4 a, f32x4 b) { return u32x4{pk(a[0], a[1]), pk(a[2], a[3]), pk(b[0], b[1]), pk(b[2], b[3])}; }

DI void role_ssm(const char* x, const u32* logA, const u32* Aim, const u32* Cre, const u32* Cim, const u16* logdt,
                 const u16* Dsk, char* ws, int cb, int wave, int lane) {
  constexpr u32 UB = 4608;
  constexpr u32 SB0 = 8 * UB, SBW = 8704, PIT = 544;
  const int chn = cb * 8 + wave, tid = wave * 64 + lane, fr = lane & 15, fh = lane >> 4;
  const u32 pa = logA[chn * 64 + lane], pi = Aim[chn * 64 + lane], pcr = Cre[chn * 64 + lane], pci = Cim[chn * 64 + lane];
  const float ldt = bf2f(logdt[chn]), dsk = bf2f(Dsk[chn]);
  u32x4 ur[4];
#pragma unroll
  for (int i = 0; i < 4; ++i) ur[i] = *(const u32x4*)(x + (4 * tid + i) * XROW + cb * 16);
  const u32 sb = SB0 + wave * SBW;
  if (lane < 32) ldsw128(wave * UB + lane * 16, u32x4{0, 0, 0, 0});
  const int xb = fr + 16 - 8 * fh;
  const u32x4 B3 = boxfrag(xb, 3, 5), B5 = boxfrag(xb, 6, 10), B7 = boxfrag(xb, 5, 11), B9 = boxfrag(xb, 4, 12);
  const float dt = rbf(__expf(ldt));
  f32x2 lr = {-rbf(__expf(bf2f(pa))) * dt, -rbf(__expf(bfhi(pa))) * dt};
  f32x2 rv = {bf2f(pi) * dt, bfhi(pi) * dt};
  lr *= 1.44269504089f; rv *= 0.15915494309f;
  const cx2 w1 = cpow(lr, rv, 1.f), w8 = cpow(lr, -rv, 8.f), wb = cpow(lr, rv, 128.f);
  const cx2 cc = {f32x2{bf2f(pcr), bfhi(pcr)} * (2.f * dt), f32x2{bf2f(pci), bfhi(pci)} * (2.f * dt)};
  auto put = [&](int row, cx2 v) { ldsw64(sb + row * PIT + lane * 8, u32x2{pk(v.r[0], v.i[0]), pk(v.r[1], v.i[1])}); };
  const u32 fa = sb + fr * PIT + fh * 16;
  u32x4 yf[8];
  {
    cx2 y = {f32x2{1.f, 1.f}, f32x2{0.f, 0.f}};
    put(0, y); y = w8; put(1, y);
#pragma unroll
    for (int r = 2; r < 16; ++r) { y = cmul(y, w8); put(r, y); }
    CFENCE();
#pragma unroll
    for (int s = 0; s < 8; ++s) yf[s] = ldsr128(fa + s * 64);
    CFENCE();
  }
  auto ktile = [&]() {
    f32x4 c = {0.f, 0.f, 0.f, 0.f};
    CFENCE();
#pragma unroll
    for (int s = 0; s < 8; ++s) c = mfma(ldsr128(fa + s * 64), yf[s], c);
    CFENCE();
    return c;
  };
  f32x4 k0, k1, k2;
  {
    cx2 v = cc, u8 = cmul(cc, wb), e[4];
#pragma unroll
    for (int a = 0; a < 8; ++a) {
      v = cmul(v, w1); u8 = cmul(u8, w1);
      put(a, v); put(8 + a, u8);
      if (a & 1) e[a >> 1] = u8;
    }
    k0 = ktile();
#pragma unroll
    for (int b = 0; b < 2; ++b)
#pragma unroll
      for (int a = 0; a < 4; ++a) { e[a] = cmul(e[a], wb); put(4 * b + a, e[a]); }
    put(8, cc);
    k1 = ktile();
#pragma unroll
    for (int b = 0; b < 4; ++b) {
      e[1] = cmul(e[1], wb); e[3] = cmul(e[3], wb);
      put(2 * b, e[1]); put(2 * b + 1, e[3]);
    }
#pragma unroll
    for (int b = 0; b < 8; ++b) { e[3] = cmul(e[3], wb); put(8 + b, e[3]); }
    k2 = ktile();
  }
  constexpr u32 TPE = 0, TPO = 1152, F2 = 2304, F2S = 144, F3 = F2 + 4 * F2S, F3S = 176, FCP = 4 * F2S + 2 * F3S;
#pragma unroll
  for (int i = 0; i < 5; ++i) ldsw128(sb + i * 1024 + lane * 16, u32x4{0, 0, 0, 0});
  CFENCE();
  {
    if (fh < 2) {
      const int P = 256 + 128 * fh + 8 * fr;
      const u32 d33 = pk(k1[3], k1[3]), d32 = pk(k1[3], k1[2]), d11 = pk(k1[1], k1[1]), d10 = pk(k1[1], k1[0]), d21 = pk(k1[2], k1[1]);
      ldsw128(sb + TPO + (536 - P) * 2, u32x4{d33, d32, d11, d10});
      const u32 e = sb + TPE + (535 - P) * 2;
      ldsw16(e, d33); ldsw32(e + 2, d33); ldsw32(e + 6, d21); ldsw32(e + 10, d11); ldsw16(e + 14, d10 >> 16);
      if (lane == 31) { ldsw16(sb + TPE + 30 * 2, d33); ldsw16(sb + TPO + 31 * 2, d33); }
    }
    if (lane == 32) { const u32 v = pk(k1[0] + dsk, 0.f); ldsw16(sb + TPE + 544 * 2, v); ldsw16(sb + TPO + 545 * 2, v); }
    const int P = 128 * (fh >> 1) + 4 * (fh & 1) + 8 * fr;
    const u32 d32 = pk(k0[3], k0[2]), d10 = pk(k0[1], k0[0]), d21 = pk(k0[2], k0[1]);
    ldsw64(sb + TPE + (540 - P) * 2, u32x2{d32, d10});
    const u32 o = sb + TPO + (541 - P) * 2;
    ldsw16(o, d32); ldsw32(o + 2, d21); ldsw16(o + 6, d10 >> 16);
    if (lane == 63) { ldsw16(sb + TPE + 287 * 2, d32); ldsw16(sb + TPO + 288 * 2, d32); }
    const u32 b2 = sb + F2 + 2 * (fr & 1) * F2S + (47 - 16 * fh - (fr >> 1)) * 2;
    const u32 b3 = sb + F3 + (fr & 1) * F3S + (63 - 32 * (fh - 2) - (fr >> 1)) * 2;
#pragma unroll
    for (int j = 0; j < 4; ++j) {
      const u32 a = fh < 2 ? b2 + (j & 1) * F2S - 16 * (j >> 1) : b3 - 16 * j;
      const u32 v = pk(k2[j], k2[j]);
      ldsw16(a, v); ldsw16(a + FCP + 2, v);
    }
  }
  const u32 k1024 = __builtin_amdgcn_readlane(pk(k2[3], k2[3]), 31);
#pragma unroll
  for (int c = 0; c < 4; ++c) {
    ldsw64((2 * c) * UB + 512 + tid * 8, u32x2{__builtin_amdgcn_perm(ur[1][c], ur[0][c], 0x05040100), __builtin_amdgcn_perm(ur[3][c], ur[2][c], 0x05040100)});
    ldsw64((2 * c + 1) * UB + 512 + tid * 8, u32x2{__builtin_amdgcn_perm(ur[1][c], ur[0][c], 0x07060302), __builtin_amdgcn_perm(ur[3][c], ur[2][c], 0x07060302)});
  }
  WAITL0();
  BAR();
  f32x4 acc[8];
#pragma unroll
  for (int i = 0; i < 8; ++i) acc[i] = f32x4{0.f, 0.f, 0.f, 0.f};
  const u32 ua = wave * UB + 512 - 32 + fr * 32 + fh * 16;
  const u32 ta = sb + ((fr & 1) ? TPO + 2 : TPE) + (528 - fr + 8 * fh) * 2;
  {
    const u32 par = (fr & 1) ? 0 : FCP + 2;
    const u32 t2 = sb + F2 + par + (47 - fr + 4 * fh) * 2, t3 = sb + F3 + par + (63 - fr + 4 * fh) * 2;
    u32x4 TA[3], TB[3], TC[4];
#pragma unroll
    for (int d = 0; d < 3; ++d) {
      TA[d] = u32x4{ldsr32(t2 - 32 * d), ldsr32(t2 - 32 * d + 4), ldsr32(t2 + F2S - 32 * d), ldsr32(t2 + F2S - 32 * d + 4)};
      TB[d] = u32x4{ldsr32(t2 + 2 * F2S - 32 * d), ldsr32(t2 + 2 * F2S - 32 * d + 4), ldsr32(t2 + 3 * F2S - 32 * d), ldsr32(t2 + 3 * F2S - 32 * d + 4)};
    }
#pragma unroll
    for (int d = 0; d < 4; ++d)
      TC[d] = u32x4{ldsr32(t3 - 32 * d), ldsr32(t3 - 32 * d + 4), ldsr32(t3 + F3S - 32 * d), ldsr32(t3 + F3S - 32 * d + 4)};
    const u32x4 F32 = lds4x32(ta - 1024);
    u32x4 FX = boxfrag(xb, 3, 4);
#pragma unroll
    for (int d = 0; d < 4; ++d) FX[d] = ((FX[d] >> 7) & 0x00010001u) * 0xffffu & k1024;
    const f32x4 zero = {0.f, 0.f, 0.f, 0.f};
#pragma unroll
    for (int b = 0; b < 6; ++b) {
      const u32x4 W0 = ldsr128(ua + b * 512), W1 = ldsr128(ua + b * 512 - 16);
      const u32x4 SA = pack2(mfma(W0, B3, zero), mfma(W0, B5, zero));
      const u32x4 SB = pack2(mfma(W1, B3, zero), mfma(W1, B5, zero));
      acc[b + 2] = mfma(F32, W0, acc[b + 2]);
#pragma unroll
      for (int d = 0; d < 3; ++d)
        if (b + 2 + d < 8) { acc[b + 2 + d] = mfma(SA, TA[d], acc[b + 2 + d]); acc[b + 2 + d] = mfma(SB, TB[d], acc[b + 2 + d]); }
      if (b < 4) {
        const u32x4 SC = pack2(mfma(W0, B7, zero), mfma(W1, B9, zero));
        acc[b + 4] = mfma(FX, W0, acc[b + 4]);
#pragma unroll
        for (int d = 0; d < 4; ++d)
          if (b + 4 + d < 8) acc[b + 4 + d] = mfma(SC, TC[d], acc[b + 4 + d]);
      }
    }
  }
#pragma unroll 2
  for (int d0 = 0; d0 < 8; ++d0) {
    const u32x4 Fa = lds4x32(ta - d0 * 64), Fb = lds4x32(ta - d0 * 64 - 512);
#pragma unroll
    for (int p = 0; p < 8; ++p) {
      const u32x4 U = ldsr128(ua + p * 512 - d0 * 64);
      acc[p] = mfma(Fa, U, acc[p]);
      if (p < 7) acc[p + 1] = mfma(Fb, U, acc[p + 1]);
    }
  }
  char* dst = ws + WS_BT + chn * 4096 + fr * 32 + fh * 8;
#pragma unroll
  for (int i = 0; i < 8; ++i) *(u32x2*)(dst + i * 512) = u32x2{pk(acc[i][0], acc[i][1]), pk(acc[i][2], acc[i][3])};
}

__global__ __launch_bounds__(512, 2) void k1(const char* __restrict__ x, const char* __restrict__ w, const char* __restrict__ wo,
                                             const u32* __restrict__ logA, const u32* __restrict__ Aim, const u32* __restrict__ Cre,
                                             const u32* __restrict__ Cim, const u16* __restrict__ logdt, const u16* __restrict__ Dsk,
                                             const char* __restrict__ gw, char* ws) {
  extern __shared__ __attribute__((aligned(16))) char smem[];
  const int xcd = blockIdx.x & 7, loc = blockIdx.x >> 3;
  const int wave = __builtin_amdgcn_readfirstlane(threadIdx.x >> 6), lane = threadIdx.x & 63;
  if (loc < 16) {
    const int rl = loc >> 2, gl = loc & 3;
    role_kv(x, w, wo, gw, ws, (xcd >> 1) * 4 + rl, (xcd & 1) * 4 + gl, (2 * (rl + gl)) % 9, wave, lane);
  } else if (loc < 25) {
    role_ssm(x, logA, Aim, Cre, Cim, logdt, Dsk, ws, xcd * 9 + loc - 16, wave, lane);
  } else {
    const int l = loc - 25, Q = xcd >> 1, jl = l >> 1;
    if (Q == 1 && jl == 2) return;
    role_q(x, w, ws, (Q == 0 ? 0 : Q == 1 ? 3 : Q == 2 ? 5 : 8) + jl, (xcd & 1) * 2 + (l & 1), (3 * jl + 4 * (l & 1) + 1) % 9, wave, lane);
  }
}

__global__ __launch_bounds__(512, 2) void k2(const char* __restrict__ ws, char* __restrict__ out) {
  extern __shared__ __attribute__((aligned(16))) char smem[];
  const int xcd = blockIdx.x & 7, loc = blockIdx.x >> 3;
  const int rb = xcd * 4 + (loc >> 3), cb = loc & 7;
  const int wave = __builtin_amdgcn_readfirstlane(threadIdx.x >> 6), lane = threadIdx.x & 63;
  const int ct0 = cb < 4 ? 5 * cb : 4 * cb + 4;
  const bool five = cb < 4;
  constexpr u32 BL = 5 * 18 * 1024;
  if (wave >= 4) {
    const int lw = wave - 4;
    const char* mb = ws + WS_M + ct0 * (18 * 1024);
#pragma unroll
    for (int gq = 0; gq < 6; ++gq) {
#pragma unroll
      for (int s = 0; s < 3; ++s) glds(mb + (lw * 18 + gq * 3 + s) * 1024, lane * 16, (lw * 18 + gq * 3 + s) * 1024);
      const int s5 = gq * 3 + (lw < 3 ? lw : 0);
      if (five && lw < 3) glds(mb + (72 + s5) * 1024, lane * 16, (72 + s5) * 1024);
      else if (gq < 3 || lw == 3) {
      }
    }
    {
      const char* bt = ws + WS_BT + (ct0 * 16) * 4096 + rb * 128;
#pragma unroll
      for (int i = 0; i < 3; ++i) {
        const int pc = lw + 4 * i;
        const int ir = 8 * pc + (lane >> 3);
        const int chn = ir < 64 ? 4 * (ir & 15) + (ir >> 4) : ir;
        if (pc < (five ? 10 : 8)) glds(bt, chn * 4096 + (((lane & 7) ^ ((ir >> 1) & 7)) << 4), BL + pc * 1024);
      }
    }
    const int per = (five && lw < 3) ? 4 : 3;
    const int nb = five ? (lw < 2 ? 3 : 2) : 2;
#define KWAIT(G)                                                                                       \
  {                                                                                                    \
    const int n = per * (5 - G) + nb;                                                                  \
    if (n >= 3 * (5 - G) + 2 + (5 - G)) asm volatile("s_waitcnt vmcnt(%0)" ::"i"(4 * (5 - G) + 2) : "memory"); \
    else asm volatile("s_waitcnt vmcnt(%0)" ::"i"(3 * (5 - G) + 2) : "memory");                        \
    BAR();                                                                                             \
  }
    KWAIT(0) KWAIT(1) KWAIT(2) KWAIT(3) KWAIT(4) KWAIT(5)
    WAITV(0);
    BAR();
    return;
  }
  const int fr = lane & 15, fh = lane >> 4;
  const char* pq = ws + WS_PQ + (rb * 4 + wave) * (18 * 1024) + lane * 16;
  u32x4 a[18];
#pragma unroll
  for (int s = 0; s < 18; ++s) a[s] = *(const u32x4*)(pq + s * 1024);
  const f32x4 g = *(const f32x4*)(ws + WS_G + (rb * 64 + wave * 16 + fh * 4) * 4);
  f32x4 acc[5];
#pragma unroll
  for (int t = 0; t < 5; ++t) acc[t] = f32x4{0.f, 0.f, 0.f, 0.f};
#pragma unroll
  for (int gq = 0; gq < 6; ++gq) {
    BAR();
#pragma unroll
    for (int s = gq * 3; s < gq * 3 + 3; ++s) {
#pragma unroll
      for (int t = 0; t < 4; ++t) acc[t] = mfma(a[s], ldsr128((t * 18 + s) * 1024 + lane * 16), acc[t]);
      if (five) acc[4] = mfma(a[s], ldsr128((72 + s) * 1024 + lane * 16), acc[4]);
    }
  }
  BAR();
  float o[5][4];
#pragma unroll
  for (int t = 0; t < 5; ++t) {
    const int ir = 16 * t + fr;
    const u32x2 bv = ldsr64(BL + ir * 128 + (((2 * wave + (fh >> 1)) ^ ((ir >> 1) & 7)) << 4) + (fh & 1) * 8);
    const float b[4] = {bf2f(bv[0]), bfhi(bv[0]), bf2f(bv[1]), bfhi(bv[1])};
#pragma unroll
    for (int j = 0; j < 4; ++j) o[t][j] = g[j] * acc[t][j] + (1.f - g[j]) * b[j];
  }
  char* dst = out + (rb * 64 + wave * 16 + fh * 4) * XROW + ct0 * 32;
#pragma unroll
  for (int j = 0; j < 4; ++j) {
    *(u32x2*)(dst + j * XROW + fr * 8) = u32x2{pk(o[0][j], o[1][j]), pk(o[2][j], o[3][j])};
    if (five) *(u16*)(dst + j * XROW + 128 + fr * 2) = (u16)pk(o[4][j], o[4][j]);
  }
}

extern "C" void kernel_launch(void* const* d_in, const int* in_sizes, int n_in, void* d_out, int out_size, void* d_ws,
                              size_t ws_size, hipStream_t stream) {
  (void)hipFuncSetAttribute((const void*)k1, hipFuncAttributeMaxDynamicSharedMemorySize, K1_LDS);
  (void)hipFuncSetAttribute((const void*)k2, hipFuncAttributeMaxDynamicSharedMemorySize, K2_LDS);
  k1<<<248, 512, K1_LDS, stream>>>((const char*)d_in[0], (const char*)d_in[1], (const char*)d_in[2], (const u32*)d_in[4],
                                   (const u32*)d_in[5], (const u32*)d_in[6], (const u32*)d_in[7], (const u16*)d_in[8],
                                   (const u16*)d_in[9], (const char*)d_in[10], (char*)d_ws);
  k2<<<256, 512, K2_LDS, stream>>>((const char*)d_ws, (char*)d_out);
}
```

```cpp
#include <hip/hip_runtime.h>
#include <stdint.h>

typedef unsigned int u32;
typedef unsigned short u16;
typedef unsigned long long u64;
typedef __attribute__((ext_vector_type(2))) unsigned u32x2;
typedef __attribute__((ext_vector_type(4))) unsigned u32x4;
typedef __attribute__((ext_vector_type(2))) float f32x2;
typedef __attribute__((ext_vector_type(4))) float f32x4;
typedef __attribute__((ext_vector_type(4))) short s16x4;
typedef __attribute__((ext_vector_type(2))) __bf16 bf16x2;
typedef __attribute__((ext_vector_type(8))) __bf16 bf16x8;
#define LAS __attribute__((address_space(3)))
#define GAS __attribute__((address_space(1)))
#define DI __device__ __forceinline__
typedef LAS char* lp;

constexpr int SEQ = 2048, DM = 576;
constexpr int XROW = DM * 2;
constexpr int WROW = 3 * DM * 2;
constexpr int WS_PQ = 0;
constexpr int WS_M = WS_PQ + 128 * 18 * 1024;
constexpr int WS_BT = WS_M + 36 * 18 * 1024;
constexpr int WS_G = WS_BT + DM * SEQ * 2;
constexpr int WS_MBKV = WS_G + SEQ * 4;
constexpr int MBKV_SZ = 16 * 36 * 16;
constexpr int WS_MBZ = WS_MBKV + 128 * MBKV_SZ;
constexpr int MBZ_SZ = 2 * 16 * 20 * 16;
constexpr int WS_EP = WS_MBZ + 44 * MBZ_SZ;
constexpr int WS_END = WS_EP + 176 * 64;
constexpr int K1_LDS = 162048;
constexpr int K2_LDS = 73728 + 10240;
#define AUX_OUT 16

DI float bf2f(u32 v) { return __uint_as_float(v << 16); }
DI float bfhi(u32 v) { return __uint_as_float(v & 0xffff0000u); }
DI u32 pk(float a, float b) { bf16x2 v = {(__bf16)a, (__bf16)b}; return __builtin_bit_cast(u32, v); }
DI float rbf(float a) { return bf2f(pk(a, a) & 0xffffu); }
DI f32x4 mfma(u32x4 a, u32x4 b, f32x4 c) {
  return __builtin_amdgcn_mfma_f32_16x16x32_bf16(__builtin_bit_cast(bf16x8, a), __builtin_bit_cast(bf16x8, b), c, 0, 0, 0);
}
DI u32x4 r128(lp a) { return *(LAS u32x4*)a; }
DI u32x2 r64(lp a) { return *(LAS u32x2*)a; }
DI u32 r32(lp a) { return *(LAS u32*)a; }
DI void w128(lp a, u32x4 v) { *(LAS u32x4*)a = v; }
DI void w64(lp a, u32x2 v) { *(LAS u32x2*)a = v; }
DI void w32(lp a, u32 v) { *(LAS u32*)a = v; }
DI void w16(lp a, u32 v) { *(LAS u16*)a = (u16)v; }
DI u32x2 rtr(lp a) { return __builtin_bit_cast(u32x2, __builtin_amdgcn_ds_read_tr16_b64_v4i16((LAS s16x4*)a)); }
DI u32x4 rtr2(lp a0, lp a1) { u32x2 l = rtr(a0), h = rtr(a1); return u32x4{l[0], l[1], h[0], h[1]}; }
DI u32x4 r4x32(lp a) { return u32x4{r32(a), r32(a + 4), r32(a + 8), r32(a + 12)}; }
DI u32x4 r2x2(lp a, lp b) { return u32x4{r32(a), r32(a + 4), r32(b), r32(b + 4)}; }
DI void glds(const void* sbase, u32 voff, u32 lds) {
  asm volatile("s_mov_b32 m0, %2\n\ts_nop 0\n\tglobal_load_lds_dwordx4 %0, %1" ::"v"(voff), "s"(sbase), "s"(lds) : "memory");
}
DI void glds4(const void* sbase, u32 lds, u32 v0, u32 v1, u32 v2, u32 v3) {
  asm volatile("s_mov_b32 m0, %5\n\ts_nop 0\n\tglobal_load_lds_dwordx4 %0, %4\n\tglobal_load_lds_dwordx4 %1, %4 offset:1024\n\t"
               "global_load_lds_dwordx4 %2, %4 offset:2048\n\tglobal_load_lds_dwordx4 %3, %4 offset:3072"
               ::"v"(v0), "v"(v1), "v"(v2), "v"(v3), "s"(sbase), "s"(lds) : "memory");
}
DI void glds2(const void* sbase, u32 lds, u32 v0, u32 v1) {
  asm volatile("s_mov_b32 m0, %3\n\ts_nop 0\n\tglobal_load_lds_dwordx4 %0, %2\n\tglobal_load_lds_dwordx4 %1, %2 offset:1024"
               ::"v"(v0), "v"(v1), "s"(sbase), "s"(lds) : "memory");
}
DI void glds3(const void* sbase, u32 lds, u32 v) {
  asm volatile("s_mov_b32 m0, %2\n\ts_nop 0\n\tglobal_load_lds_dwordx4 %0, %1\n\tglobal_load_lds_dwordx4 %0, %1 offset:1024\n\t"
               "global_load_lds_dwordx4 %0, %1 offset:2048" ::"v"(v), "s"(sbase), "s"(lds) : "memory");
}
DI void glds_lo(const void* sbase, u32 voff, u32 lds) {
  asm volatile("s_mov_b32 m0, %2\n\ts_mov_b32 exec_hi, 0\n\tglobal_load_lds_dwordx4 %0, %1\n\ts_mov_b32 exec_hi, -1"
               ::"v"(voff), "s"(sbase), "s"(lds) : "memory");
}
template <int N> DI void waitv() { asm volatile("s_waitcnt vmcnt(%0)" ::"i"(N) : "memory"); }
#define CFENCE() asm volatile("" ::: "memory")
#define WAITL0() do { __builtin_amdgcn_s_waitcnt(0xC07F); CFENCE(); } while (0)
#define BAR() do { CFENCE(); __builtin_amdgcn_s_barrier(); CFENCE(); } while (0)
#define SCHED() __builtin_amdgcn_sched_barrier(0)
#define PIN(x) asm volatile("" : "+v"(x))
#define PINS(x) asm volatile("" ::"s"(x))
constexpr float EPS = 1e-6f, L2E = 1.44269504089f;
DI f32x2 phi2(float a0, float a1) {
  f32x2 m = f32x2{a0, a1} * L2E - EPS * L2E;
  f32x2 e = {__builtin_amdgcn_fmed3f(__builtin_amdgcn_exp2f(m[0]), 0.f, 1.f), __builtin_amdgcn_fmed3f(__builtin_amdgcn_exp2f(m[1]), 0.f, 1.f)};
  const int ei = __builtin_bit_cast(int, EPS);
  f32x2 x = {__int_as_float(max(__float_as_int(a0), ei)), __int_as_float(max(__float_as_int(a1), ei))};
  return x + e;
}
DI f32x4 phi4(f32x4 a) { f32x2 l = phi2(a[0], a[1]), h = phi2(a[2], a[3]); return f32x4{l[0], l[1], h[0], h[1]}; }
DI float rowsum16(float v) {
  v += __builtin_bit_cast(float, __builtin_amdgcn_update_dpp(0, __builtin_bit_cast(int, v), 0x128, 0xf, 0xf, false));
  v += __builtin_bit_cast(float, __builtin_amdgcn_update_dpp(0, __builtin_bit_cast(int, v), 0x124, 0xf, 0xf, false));
  v += __builtin_bit_cast(float, __builtin_amdgcn_update_dpp(0, __builtin_bit_cast(int, v), 0x122, 0xf, 0xf, false));
  v += __builtin_bit_cast(float, __builtin_amdgcn_update_dpp(0, __builtin_bit_cast(int, v), 0x121, 0xf, 0xf, false));
  return v;
}
DI float prodsum(float a, float b, float c, float d) {
  auto s0 = __builtin_amdgcn_permlane32_swap(__float_as_uint(a), __float_as_uint(c), false, false);
  auto s1 = __builtin_amdgcn_permlane32_swap(__float_as_uint(b), __float_as_uint(d), false, false);
  const float ac = __uint_as_float(s0[0]) + __uint_as_float(s0[1]), bd = __uint_as_float(s1[0]) + __uint_as_float(s1[1]);
  auto s2 = __builtin_amdgcn_permlane16_swap(__float_as_uint(ac), __float_as_uint(bd), false, false);
  float v = __uint_as_float(s2[0]) + __uint_as_float(s2[1]);
  v += __builtin_bit_cast(float, __builtin_amdgcn_update_dpp(0, __builtin_bit_cast(int, v), 0x128, 0xf, 0xf, false));
  v += __builtin_bit_cast(float, __builtin_amdgcn_update_dpp(0, __builtin_bit_cast(int, v), 0x124, 0xf, 0xf, false));
  return v;
}
DI float prodsum(u32x4 m) { return prodsum(bf2f(m[0]), bfhi(m[0]), bf2f(m[2]), bfhi(m[2])); }
DI u32x4 pack2(f32x4 a, f32x4 b) { return u32x4{pk(a[0], a[1]), pk(a[2], a[3]), pk(b[0], b[1]), pk(b[2], b[3])}; }
typedef __amdgpu_buffer_rsrc_t rsrc_t;
DI rsrc_t wsrsrc(char* ws) { return __builtin_amdgcn_make_buffer_rsrc(ws, 0, WS_END, 0x00020000); }
DI void st_sc1(rsrc_t r, u32 off, u32x4 v) { __builtin_amdgcn_raw_buffer_store_b128(v, r, off, 0, 16); }
DI u32x4 ld_sc1(rsrc_t r, u32 off) { return __builtin_amdgcn_raw_buffer_load_b128(r, off, 0, 16); }
DI void st32_sc1(rsrc_t r, u32 off, u32 v) { __builtin_amdgcn_raw_buffer_store_b32(v, r, off, 0, 16); }
DI u32 ld_ep(char* ws, int i) { return __hip_atomic_load((GAS u32*)(ws + WS_EP) + 16 * i, __ATOMIC_RELAXED, __HIP_MEMORY_SCOPE_AGENT); }
DI void st_ep(rsrc_t r, int i, int lane, u32 v) { if (lane < 4) st_sc1(r, WS_EP + 64 * i + 16 * lane, u32x4{v, v, v, v}); }
constexpr u32 OOR = 0x7ffffff0u;
constexpr u32 SPIN_CAP = 1u << 15;
#define POLL_SLEEP 4
#define STDECL
#define STARG
#define STPASS
#define STAMP(i)
#define STFLUSH(k)

constexpr int XR_KV = 4 * 18432, XR_Q = 3 * 18432;
constexpr int GWL = XR_KV + 4 * 16384, GST = GWL + 1152;
constexpr int WOI = GWL + 1280;
constexpr int XCH = XR_KV + 16384, MSG = 18432, KVA = MSG + 4096;

template <int ROLE>
DI void gemm_main(lp L, int rg, int lane, f32x4 (&acc)[ROLE ? 3 : 2][9] STARG) {
  constexpr int RT = ROLE ? 3 : 2, XB = RT * 8192, XRING = ROLE ? XR_Q : XR_KV, NS = ROLE ? 3 : 4;
  const int fr = lane & 15, fh = lane >> 4;
  const int q4 = (lane >> 2) & 3, p4 = lane & 3;
  const int ax0 = XRING + (rg * RT * 16 + fr) * 128 + ((fh ^ ((fr >> 1) & 7)) << 4), ax1 = ax0 ^ 64;
  const int r0 = (8 * fh + q4 + 4 * (fh & 1)) * 288, r1 = (8 * fh + q4 + 4 * ((fh & 1) ^ 1)) * 288;
  const int cm = ROLE ? 72 * p4 : 8 * p4, cx = 72 * p4 + 32;
  u32x4 a0[RT], a1[RT], b0[9], b1[9];
  lp W0 = L + r0 + cm, W1 = L + r1 + cm, E0 = L + r0 + cx, E1 = L + r1 + cx, X0 = L + ax0, X1 = L + ax1;
  PIN(E0); PIN(E1);
  auto step = [&](bool domma, u32x4 (&a)[RT], u32x4 (&b)[9], int slot, int s, u32x4 (&an)[RT], u32x4 (&bn)[9]) {
    auto rd = [&](int n) {
      if (n < RT) { an[n] = r128((s ? X1 : X0) + slot * XB + n * 2048); return; }
      n -= RT;
      if (n >= 18) return;
      const int t = n >> 1, so = slot * 18432 + s * 9216;
      const int to = (ROLE ? 8 * t : 72 * (t >> 1) + 40 * (t & 1)) + so;
      const u32x2 v = (ROLE || t < 8) ? rtr((n & 1 ? W1 : W0) + to) : rtr((n & 1 ? E1 : E0) + so);
      bn[t][2 * (n & 1)] = v[0]; bn[t][2 * (n & 1) + 1] = v[1];
    };
    const int EX = ROLE ? 0 : (s ? 7 : 2);
#pragma unroll
    for (int m = 0; m < 9 * RT; ++m) {
      const int t = m / RT, i = m % RT;
      if (domma) acc[i][t] = ROLE ? mfma(b[t], a[i], acc[i][t]) : mfma(a[i], b[t], acc[i][t]);
      SCHED();
      if (m < 2 * EX) { rd(m + (m + 1) / 2); if (!(m & 1)) rd(m + m / 2 + 1); } else rd(m + EX);
      SCHED();
    }
  };
#pragma unroll
  for (int i = 0; i < RT; ++i)
#pragma unroll
    for (int t = 0; t < 9; ++t) { const float v = (ROLE || t < 4 || (t == 8 && fr < 8)) ? EPS : 0.f; acc[i][t] = f32x4{v, v, v, v}; PIN(acc[i][t]); }
  STAMP(1);
  BAR();
  STAMP(2);
  step(false, a1, b1, 0, 0, a0, b0);
#pragma unroll 1
  for (int it = 0; it < (ROLE ? 3 : 2); ++it) {
#pragma unroll
    for (int sl = 0; sl < NS; ++sl) {
      step(true, a0, b0, sl, 1, a1, b1);
      WAITL0();
      BAR();
      SCHED();
      step(true, a1, b1, sl == NS - 1 ? 0 : sl + 1, 0, a0, b0);
    }
  }
  if (ROLE == 0) {
    step(true, a0, b0, 0, 1, a1, b1);
#pragma unroll
    for (int t = 0; t < 9; ++t)
#pragma unroll
      for (int i = 0; i < RT; ++i) acc[i][(t + 8) % 9] = mfma(a1[i], b1[(t + 8) % 9], acc[i][(t + 8) % 9]);
  }
}

DI void kv_consume(lp L, rsrc_t rs, int rb, int g, u32 tme, int wave, int lane STARG) {
  const int chead = 4 * g + (rb & 3), crole = rb >> 2;
  if (crole == 3) return;
  const int fr = lane & 15, fh = lane >> 4;
  const bool ext = crole == 2;
  u32x4 wf[6];
  {
    const int codd = chead & 1, q4 = (lane >> 2) & 3, p4 = lane & 3;
    int s0 = 8 * fh + q4, s1 = s0 + 4;
    s0 = s0 > 17 ? 17 : s0; s1 = s1 > 17 ? 17 : s1;
    s0 += 2 * codd; s1 += 2 * codd;
    s0 = s0 >= 18 ? s0 - 18 : s0; s1 = s1 >= 18 ? s1 - 18 : s1;
    lp p0 = L + WOI + s0 * XROW + wave * 64 + p4 * 8, p1 = L + WOI + s1 * XROW + wave * 64 + p4 * 8;
#pragma unroll
    for (int i = 0; i < 6; ++i) wf[i] = rtr2(p0 + (i >> 1) * 512 + (i & 1) * 32, p1 + (i >> 1) * 512 + (i & 1) * 32);
  }
  const int un = 2 * (chead >> 3) + (chead & 1), hh = (chead >> 1) & 3;
  const int col = (wave & 1) * 32 + (lane & 31);
  u32 mo = WS_M + (wave >> 1) * 73728 + (ext ? 65536 + (un * 4 + hh) * 256 + col * 4 : (2 * un + crole) * 4096 + hh * 1024 + ((col ^ (col >> 4)) << 4));
  if (lane >= 32) mo = OOR;
  PIN(mo);
  STAMP(6);
  if (wave < (ext ? 3 : 8)) {
    const int it = 4 * wave + (lane & 3);
    const u32 off = WS_MBKV + (g * 16 + rb) * MBKV_SZ + (lane >> 2) * 576 + it * 16;
    const bool two = wave == 0 && !ext;
    lp d = L + KVA + (ext ? (fh & 1) * 64 + it * 4 + (fh >> 1) * 2 : ((it & 1) * 4 + fh) * 64 + (it >> 1) * 2);
    PIN(d);
    u32x4 m, e = {0, tme, 0, tme};
    for (u32 spin = 0;; ++spin) {
      m = ld_sc1(rs, off);
      if (two) e = ld_sc1(rs, off + 512);
      bool ok = m[1] == tme && m[3] == tme && e[1] == tme && e[3] == tme;
      if (__all(ok) || spin > SPIN_CAP) break;
      __builtin_amdgcn_s_sleep(POLL_SLEEP);
    }
    STAMP(7);
    const float sm = prodsum(m);
    if (fr < 4 && !(ext && it > 8)) w16(d, pk(sm, sm));
    if (two) { const float se = prodsum(e); if (fr < 4) w16(d + 32, pk(se, se)); }
  }
  WAITL0();
  BAR();
  STAMP(8);
  const u32x4 af = r128(L + KVA + fr * 64 + fh * 16);
#pragma unroll
  for (int i = 0; i < 3; ++i) {
    if (i == 2 && wave >= 2) break;
    const f32x4 u = mfma(af, wf[2 * i], f32x4{0.f, 0.f, 0.f, 0.f}), v = mfma(af, wf[2 * i + 1], f32x4{0.f, 0.f, 0.f, 0.f});
    auto sx = __builtin_amdgcn_permlane16_swap(pk(u[0], u[1]), pk(v[0], v[1]), false, false);
    auto sy = __builtin_amdgcn_permlane16_swap(pk(u[2], u[3]), pk(v[2], v[3]), false, false);
    if (ext) st32_sc1(rs, mo + i * (4 * 73728), sx[0]);
    else st_sc1(rs, mo + i * (4 * 73728), u32x4{sx[0], sy[0], sx[1], sy[1]});
  }
  if (wave == 7) st_ep(rs, g * 16 + rb, lane, tme);
}

DI void role_load(lp L, const char* x, const char* w, const char* wo_g, const char* gw, char* ws, bool isq, int rbi, int cg, int c0,
                  int lw, int lane STARG) {
  __builtin_amdgcn_s_setprio(2);
  const bool six = isq && rbi < 10;
  const int XP = isq ? 6 : 4;
  const char* xr = x + rbi * (isq ? 192 * XROW : 128 * XROW);
  const u32 xl = (isq ? XR_Q : XR_KV) + lw * XP * 1024, XB = XP * 4096;
  u32 xo[4], wo[5];
  xo[0] = (8 * (six || !isq ? lw * XP : (lw * XP > 12 ? 12 : lw * XP)) + (lane >> 3)) * XROW + (((lane & 7) ^ (lane >> 4)) << 4);
  xo[1] = (xo[0] ^ 64) + 8192; xo[2] = xo[0] + 16384; xo[3] = xo[1] + 16384;
  auto issx = [&](int cidx, int slot) {
    const char* xb = xr + cidx * 128;
    glds4(xb, xl + slot * XB, xo[0], xo[1], xo[2], xo[3]);
    if (isq) glds2(xb + (six || lw < 2 ? 32 * XROW : 0), xl + slot * XB + 4096, xo[0], xo[1]);
  };
  auto issw = [&](int cidx, int slot) {
    const char* wb = w + cidx * (64 * WROW);
    const u32 lb = slot * 18432 + lw * 4608;
    glds4(wb, lb, wo[0], wo[1], wo[2], wo[3]);
    glds_lo(wb, wo[4], lb + 4096);
  };
  auto nxt = [](int c) { return c == 8 ? 0 : c + 1; };
  STAMP(3);
  issx(c0, 0);
  SCHED();
  {
    const int cb0 = (isq ? 288 * cg : 1152 + 144 * cg) + 16 * lw * WROW, cb1 = isq ? cb0 : cb0 + 1008;
#pragma unroll
    for (int i = 0; i < 5; ++i) {
      const int f = i < 4 ? 64 * i + lane : 256 + (lane & 31);
      const int lr = (f * 3641) >> 16, cc = f - lr * 18;
      wo[i] = (lr ^ ((lr >> 1) & 4)) * WROW + (cc < 9 ? cb0 : cb1) + 16 * cc - (i < 4 ? 1024 * i : 0);
    }
  }
  issw(c0, 0);
  STAMP(9);
  int c = nxt(c0);
  const int fr = lane & 15, fh = lane >> 4;
  const int me = isq ? 128 + cg * 11 + rbi : cg * 16 + rbi;
  const int jq = lane >> 2, cz = (cg >> 1) * 11 + (jq > 10 ? 10 : jq);
  float gl = 0.f;
  u32 tme, tz;
  if (isq) {
    issx(c, 1); issw(c, 1);
    c = nxt(c);
    int sx = 2;
    STAMP(1);
    waitv<11>();
    BAR();
    STAMP(2);
#pragma unroll 1
    for (int k = 0; k < 7; ++k) {
      issx(c, sx); issw(c, sx);
      c = nxt(c);
      sx = sx == 2 ? 0 : sx + 1;
      waitv<11>();
      BAR();
    }
    waitv<0>();
    tme = ld_ep(ws, me); tz = 0;
    BAR();
  } else {
    if (lane < 18) glds(gw, (lw * 18 + lane) * 16, GWL + lw * 288);
    issx(c, 1); issw(c, 1);
    c = nxt(c);
    issx(c, 2);
    const int grow = 16 * cg + 4 * lw + fh;
    lp gx = L + XR_KV + grow * 128 + ((((lane >> 1) & 7) ^ ((grow >> 1) & 7)) << 4) + (lane & 1) * 8;
    lp gg = L + GWL + fr * 8;
    u32x2 ga, gb;
    int cur = c0;
    auto gate_rd = [&](int k) { ga = r64(gx + (k & 3) * 16384); gb = r64(gg + cur * 128); cur = nxt(cur); };
    auto gate_acc = [&]() {
      const u32 a0 = ga[0], a1 = ga[1], b0 = gb[0], b1 = gb[1];
      gl = __builtin_amdgcn_fdot2_f32_bf16(__builtin_bit_cast(bf16x2, a0), __builtin_bit_cast(bf16x2, b0), gl, false);
      gl = __builtin_amdgcn_fdot2_f32_bf16(__builtin_bit_cast(bf16x2, a1), __builtin_bit_cast(bf16x2, b1), gl, false);
      PIN(gl);
    };
    STAMP(1);
    waitv<13>();
    BAR();
    STAMP(2);
#pragma unroll 1
    for (int k = 0; k < 6; ++k) {
      issw(c, (k + 2) & 3);
      gate_rd(k);
      c = nxt(c);
      issx(c, (k + 3) & 3);
      gate_acc();
      waitv<13>();
      BAR();
    }
    issw(c, 0);
    gate_rd(6);
    if (rbi < 12) {
      const char* src = wo_g + (4 * cg + (rbi & 3)) * (18 * XROW) + lw * 5184;
      glds4(src, WOI + lw * 5184, lane * 16, lane * 16, lane * 16, lane * 16);
      glds(src + 4096, lane * 16, WOI + lw * 5184 + 4096);
      if (lane < 4) glds(src + 5120, lane * 16, WOI + lw * 5184 + 5120);
      gate_acc();
      waitv<15>(); BAR();
      gate_rd(7); gate_acc();
      waitv<6>();
    } else {
      gate_acc();
      waitv<9>(); BAR();
      gate_rd(7); gate_acc();
      waitv<0>();
    }
    tme = ld_ep(ws, me); tz = ld_ep(ws, 128 + cz);
    BAR();
    gate_rd(8); gate_acc();
  }
  const rsrc_t rs = wsrsrc(ws);
  tme += 1; tz += 1;
  if (isq) {
    BAR();
    const int i0 = lw * 4 + (lane & 3);
    const u32 base = WS_MBZ + (me - 128) * MBZ_SZ + (lane >> 2) * 320;
    u32 o[3]; lp zw[3];
#pragma unroll
    for (int i = 0; i < 3; ++i) {
      const int it = i0 + 16 * i, gi = it >= 20, r = it - 20 * gi;
      o[i] = base + gi * (MBZ_SZ / 2) + r * 16;
      zw[i] = L + GWL + (r < 16 ? (2 * gi + (r >> 3)) * 160 + (r & 7) * 16 : (2 * gi + (r & 1)) * 160 + 128 + (r & 2) * 8) + fh * 4;
    }
    u32x4 m[3];
    m[2] = u32x4{0, tme, 0, tme};
    for (u32 spin = 0;; ++spin) {
      bool ok = true;
      m[0] = ld_sc1(rs, o[0]); m[1] = ld_sc1(rs, o[1]);
      if (lw < 2) m[2] = ld_sc1(rs, o[2]);
#pragma unroll
      for (int i = 0; i < 3; ++i) ok = ok && m[i][1] == tme && m[i][3] == tme;
      if (__all(ok) || spin > SPIN_CAP) break;
      __builtin_amdgcn_s_sleep(POLL_SLEEP);
    }
    STAMP(7);
#pragma unroll
    for (int i = 0; i < 3; ++i) {
      if (i == 2 && lw >= 2) break;
      const float sv = prodsum(m[i]);
      if (fr < 4) w32(zw[i], __float_as_uint(sv));
    }
    WAITL0();
    BAR();
    if (lw == 0) st_ep(rs, me, lane, tme);
    return;
  }
  __builtin_amdgcn_s_setprio(0);
  if (lw == 0) w128(L + KVA + lane * 16, u32x4{0, 0, 0, 0});
  {
    const float gs = rbf(rowsum16(gl));
    if (fr == 0) w32(L + GST + (4 * lw + fh) * 4, __float_as_uint(rbf(__builtin_amdgcn_rcpf(1.f + __builtin_amdgcn_exp2f(-L2E * gs)))));
  }
  u32 zo = jq < 11 ? WS_MBZ + cz * MBZ_SZ + (cg & 1) * (MBZ_SZ / 2) + rbi * 320 + lw * 64 + (lane & 3) * 16 : OOR;
  PIN(zo);
  waitv<0>();
  WAITL0();
  BAR();
  STAMP(4);
  {
    const u32x4 one = {0x3f803f80u, 0x3f803f80u, 0x3f803f80u, 0x3f803f80u};
#pragma unroll
    for (int k = 0; k < 2; ++k) {
      if (k && lw) break;
      lp bk = L + XCH + lane * 16 + (k ? 8192 : lw * 1024);
      u32x4 kf[4];
#pragma unroll
      for (int r = 0; r < 4; ++r) kf[r] = r128(bk + r * 9216);
      const f32x4 t = mfma(kf[1], one, mfma(kf[0], one, f32x4{0.f, 0.f, 0.f, 0.f})) + mfma(kf[3], one, mfma(kf[2], one, f32x4{0.f, 0.f, 0.f, 0.f}));
      const u32 p0 = __builtin_amdgcn_ds_bpermute((lane & 3) * 64, pk(t[0], t[1])), p1 = __builtin_amdgcn_ds_bpermute((lane & 3) * 64, pk(t[2], t[3]));
      st_sc1(rs, zo + k * 256, u32x4{p0, tz, p1, tz});
    }
    if (lw == 1 && lane < 4) st_sc1(rs, WS_G + (rbi * 128 + 16 * cg) * 4 + lane * 16, r128(L + GST + lane * 16));
  }
  STAMP(5);
  kv_consume(L, rs, rbi, cg, tme, lw, lane STPASS);
}

DI void role_kv(lp L, char* ws, int rb, int g, int hi, int lane STARG) {
  const int fr = lane & 15, fh = lane >> 4;
  const rsrc_t rs = wsrsrc(ws);
  const int c0 = g * 16 + hi + (lane < 36 ? 0 : 4), c1 = g * 16 + hi + (lane < 8 ? 4 : 8);
  u32 d0 = WS_MBKV + c0 * MBKV_SZ + rb * 576 + (lane < 36 ? lane : lane - 36) * 16;
  u32 d1 = lane < 20 ? WS_MBKV + c1 * MBKV_SZ + rb * 576 + (lane < 8 ? lane + 28 : lane - 8) * 16 : OOR;
  u32 t0 = ld_ep(ws, c0), t1 = ld_ep(ws, c1), tme = ld_ep(ws, g * 16 + rb);
  PIN(d0); PIN(d1);
  f32x4 acc[2][9];
  gemm_main<0>(L, hi, lane, acc STPASS);
  STAMP(3);
  {
    lp xw = L + XCH + hi * 9216 + lane * 16;
#pragma unroll
    for (int t = 0; t < 4; ++t) {
      w128(xw + t * 1024, pack2(phi4(acc[0][t]), phi4(acc[1][t])));
      w128(xw + 4096 + t * 1024, pack2(acc[0][4 + t], acc[1][4 + t]));
    }
    const u32x4 xp = pack2(phi4(acc[0][8]), phi4(acc[1][8])), xr = pack2(acc[0][8], acc[1][8]);
    w128(xw + 8192, fr < 8 ? xp : xr);
  }
  t0 += 1; t1 += 1; tme += 1;
  lp mb = L + MSG + hi * 1024;
  WAITL0();
  BAR();
  STAMP(4);
  {
    f32x4 p1 = {0.f, 0.f, 0.f, 0.f}, p2 = p1, p3 = p1, p4 = p1;
    lp base = L + XCH + lane * 16;
    u32x4 xf[4], vf[4], kf[4];
#pragma unroll
    for (int r = 0; r < 4; ++r) { kf[r] = r128(base + hi * 1024 + r * 9216); vf[r] = r128(base + 4096 + hi * 1024 + r * 9216); xf[r] = r128(base + r * 9216 + 8192); }
#pragma unroll
    for (int r = 0; r < 4; ++r) {
      p1 = mfma(kf[r], vf[r], p1); p2 = mfma(kf[r], xf[r], p2);
      p3 = mfma(xf[r], vf[r], p3); p4 = mfma(xf[r], xf[r], p4);
    }
    const bool own = (fr >> 1) == 4 + hi;
    lp w1 = mb + (fh >> 1) * 288 + (fh & 1) * 8;
    w64(w1 + fr * 16, u32x2{pk(p1[0], p1[1]), pk(p1[2], p1[3])});
    if (own) w64(w1 + 256 + (fr & 1) * 16, u32x2{pk(p2[0], p2[1]), pk(p2[2], p2[3])});
    if (fh == (hi >> 1)) {
      w32(mb + 576 + fr * 4, (hi & 1) ? pk(p3[2], p3[3]) : pk(p3[0], p3[1]));
      if (own) w32(mb + 640 + (fr & 1) * 4, (hi & 1) ? pk(p4[2], p4[3]) : pk(p4[0], p4[1]));
    }
    CFENCE();
    const u32x2 q0 = r64(mb + lane * 8), q1 = r64(mb + 512 + lane * 8);
    st_sc1(rs, d0, u32x4{q0[0], t0, q0[1], t0});
    st_sc1(rs, d1, u32x4{q1[0], t1, q1[1], t1});
  }
  STAMP(5);
  kv_consume(L, rs, rb, g, tme, 4 + hi, lane STPASS);
}

DI void role_q(lp L, char* ws, int jq, int hg, int rg, int lane STARG) {
  const int fh = lane >> 4;
  const rsrc_t rs = wsrsrc(ws);
  f32x4 acc[3][9];
  gemm_main<1>(L, rg, lane, acc STPASS);
  STAMP(3);
#pragma unroll
  for (int i = 0; i < 3; ++i)
#pragma unroll
    for (int t = 0; t < 9; ++t) { acc[i][t] = phi4(acc[i][t]); PIN(acc[i][t]); }
  const int rt0 = jq * 12 + rg * 3;
  u32 po = WS_PQ + rt0 * (18 * 1024) + hg * 4096 + lane * 16;
  u32 pe = WS_PQ + (rt0 + (lane >> 5)) * (18 * 1024) + 16384 + hg * 512 + (lane & 31) * 16;
  PIN(po); PIN(pe);
  STAMP(7);
  BAR();
  STAMP(8);
  f32x4 z[9];
#pragma unroll
  for (int t = 0; t < 9; ++t) z[t] = __builtin_bit_cast(f32x4, r128(L + GWL + fh * 160 + t * 16));
  u32x2 ex[3];
#pragma unroll
  for (int i = 0; i < 3; ++i) {
    const f32x4 x4 = acc[i][4] * z[8];
    f32x4 d4 = acc[i][0] * z[0] + acc[i][1] * z[1] + acc[i][2] * z[2] + acc[i][3] * z[3];
    f32x4 e4 = acc[i][5] * z[4] + acc[i][6] * z[5] + acc[i][7] * z[6] + acc[i][8] * z[7];
    f32x2 d2 = f32x2{d4[0], d4[1]} + f32x2{d4[2], d4[3]} + f32x2{x4[0], x4[1]};
    f32x2 e2 = f32x2{e4[0], e4[1]} + f32x2{e4[2], e4[3]} + f32x2{x4[2], x4[3]};
    const float r = __builtin_amdgcn_rcpf(d2[0] + d2[1] + EPS), s = __builtin_amdgcn_rcpf(e2[0] + e2[1] + EPS);
    const u32 p = rt0 + i < 128 ? po + i * (18 * 1024) : OOR;
    st_sc1(rs, p, pack2(acc[i][0] * r, acc[i][1] * r));
    st_sc1(rs, p + 1024, pack2(acc[i][2] * r, acc[i][3] * r));
    st_sc1(rs, p + 2048, pack2(acc[i][5] * s, acc[i][6] * s));
    st_sc1(rs, p + 3072, pack2(acc[i][7] * s, acc[i][8] * s));
    ex[i] = u32x2{pk(acc[i][4][0] * r, acc[i][4][1] * r), pk(acc[i][4][2] * s, acc[i][4][3] * s)};
    SCHED();
  }
#pragma unroll
  for (int k = 0; k < 2; ++k) {
    const int ib = k ? 2 : 1;
    auto sx = __builtin_amdgcn_permlane32_swap(ex[2 * k][0], ex[ib][0], false, false);
    auto sy = __builtin_amdgcn_permlane32_swap(ex[2 * k][1], ex[ib][1], false, false);
    const bool ok = rt0 + 2 * k + (lane >> 5) < 128 && (k == 0 || lane < 32);
    st_sc1(rs, ok ? pe + k * (36 * 1024) : OOR, u32x4{sx[0], sy[0], sx[1], sy[1]});
  }
}

struct cx2 { f32x2 r, i; };
DI cx2 cmul(cx2 a, cx2 b) { return cx2{a.r * b.r - a.i * b.i, a.r * b.i + a.i * b.r}; }
DI cx2 cpow(f32x2 lr, f32x2 rv, float n) {
  cx2 o;
#pragma unroll
  for (int m = 0; m < 2; ++m) {
    float mag = __builtin_amdgcn_exp2f(lr[m] * n), ph = __builtin_amdgcn_fractf(rv[m] * n);
    o.r[m] = mag * __builtin_amdgcn_cosf(ph);
    o.i[m] = mag * __builtin_amdgcn_sinf(ph);
  }
  return o;
}
DI u32x4 boxfrag(int xb, int lo, int hi, u32 one) {
  u32x4 o;
#pragma unroll
  for (int d = 0; d < 4; ++d) {
    u32 a = (u32)(xb - 2 * d - lo) <= (u32)(hi - lo) ? one : 0u;
    u32 b = (u32)(xb - 2 * d - 1 - lo) <= (u32)(hi - lo) ? one << 16 : 0u;
    o[d] = a | b;
  }
  return o;
}

DI void role_ssm(lp L, const char* x, const u32* logA, const u32* Aim, const u32* Cre, const u32* Cim, const u16* logdt,
                 const u16* Dsk, char* ws, int cb, int wave, int lane STARG) {
  constexpr int UB = 4608;
  constexpr int SB0 = 8 * UB, SBW = 8704, PIT = 544, TB0 = SB0 + 8 * SBW, TBW = 4352, BOX = TB0 + 8 * TBW - 128, FLG = BOX + 5120;
  static_assert(FLG + 16 <= K1_LDS, "lds");
  const int fr = lane & 15, fh = lane >> 4;
  const int chn = cb * 8 + wave, tid = wave * 64 + lane;
  const u32 pa = logA[chn * 64 + lane], pi = Aim[chn * 64 + lane], pcr = Cre[chn * 64 + lane], pci = Cim[chn * 64 + lane];
  const float ldt = bf2f(logdt[chn]), dsk = bf2f(Dsk[chn]);
  u32x4 ur[4];
  const char* xc = x + cb * 16;
  auto ldu = [&](int i) { SCHED(); ur[i] = *(const u32x4*)(xc + (u32)(4 * tid + i) * XROW); SCHED(); };
  lp sb = L + SB0 + wave * SBW, tb = L + TB0 + wave * TBW;
#pragma unroll
  for (int i = 0; i < 4; ++i) w128(tb + i * 1024 + lane * 16, u32x4{0, 0, 0, 0});
  if (lane < 8) w128(tb + 4096 + lane * 16, u32x4{0, 0, 0, 0});
  if (lane < 32) w128(L + wave * UB + lane * 16, u32x4{0, 0, 0, 0});
  if (wave < 5) {
    const int lo = wave == 1 ? 6 : wave == 2 ? 5 : wave == 3 ? 4 : 3, hi = wave == 0 ? 5 : wave == 4 ? 4 : 9 + wave;
    w128(L + BOX + wave * 1024 + lane * 16, boxfrag(fr + 16 - 8 * fh, lo, hi, wave == 4 ? 0xffffu : 0x3f80u));
  }
  constexpr int TPE = 0, TPO = 1152, F2 = 2304, F2S = 144, F3 = F2 + 4 * F2S, F3S = 176, FCP = 4 * F2S + 2 * F3S;
  lp e0 = tb - 2 * (128 * (fh >> 1) + 4 * (fh & 1) + 8 * fr), e1 = tb - 16 * lane;
  lp e2 = tb + (lane == 31 ? 60 : lane == 63 ? 574 : 1088);
  lp fw[4];
  {
    lp b2 = tb + F2 + 2 * (fr & 1) * F2S + (47 - 16 * fh - (fr >> 1)) * 2;
    lp b3 = tb + F3 + (fr & 1) * F3S + (63 - 32 * (fh - 2) - (fr >> 1)) * 2;
#pragma unroll
    for (int j = 0; j < 4; ++j) { fw[j] = fh < 2 ? b2 + (j & 1) * F2S - 16 * (j >> 1) : b3 - 16 * j; PIN(fw[j]); }
  }
  lp ua = L + wave * UB + fr * 32 + fh * 16;
  lp ta = tb + ((fr & 1) ? TPO + 2 : TPE) + (16 - fr + 8 * fh) * 2;
  lp t2 = tb + F2 + ((fr & 1) ? 0 : FCP + 2) + (15 - fr + 4 * fh) * 2;
  lp fa = sb + fr * PIT + fh * 16, pw = sb + lane * 8;
  PIN(e0); PIN(e1); PIN(e2); PIN(ua); PIN(ta); PIN(t2); PIN(fa); PIN(pw);
  ldu(0);
  STAMP(1);
  const float dt = rbf(__expf(ldt));
  f32x2 lr = {-rbf(__expf(bf2f(pa))) * dt, -rbf(__expf(bfhi(pa))) * dt};
  f32x2 rv = {bf2f(pi) * dt, bfhi(pi) * dt};
  lr *= L2E; rv *= 0.15915494309f;
  const cx2 w1 = cpow(lr, rv, 1.f), w8 = cpow(lr, -rv, 8.f), wb = cpow(lr, rv, 128.f);
  const cx2 cc = {f32x2{bf2f(pcr), bfhi(pcr)} * (2.f * dt), f32x2{bf2f(pci), bfhi(pci)} * (2.f * dt)};
  ldu(1);
  STAMP(2);
  auto put = [&](int row, cx2 v) { w64(pw + row * PIT, u32x2{pk(v.r[0], v.i[0]), pk(v.r[1], v.i[1])}); };
  auto get = [&](u32x4 (&f)[8]) {
    CFENCE();
#pragma unroll
    for (int s = 0; s < 8; ++s) f[s] = r128(fa + s * 64);
    CFENCE();
    SCHED();
  };
  const f32x4 zero = {0.f, 0.f, 0.f, 0.f};
  u32x4 yf[8], xf[8];
  {
    cx2 y = {f32x2{1.f, 1.f}, f32x2{0.f, 0.f}};
    put(0, y); y = w8; put(1, y);
#pragma unroll
    for (int r = 2; r < 16; ++r) { y = cmul(y, w8); put(r, y); if (r == 7) ldu(2); }
    get(yf);
    ldu(3);
  }
  cx2 e[4];
  {
    cx2 v = cc, u8 = cmul(cc, wb);
#pragma unroll
    for (int a = 0; a < 8; ++a) {
      v = cmul(v, w1); u8 = cmul(u8, w1);
      put(a, v); put(8 + a, u8);
      if (a & 1) e[a >> 1] = u8;
    }
    get(xf);
  }
  STAMP(5);
  f32x4 k0 = zero, k1 = zero, k2 = zero, k2b = zero;
#pragma unroll
  for (int n = 0; n < 8; ++n) {
    k0 = mfma(xf[n], yf[n], k0);
    e[n & 3] = cmul(e[n & 3], wb); put(n, e[n & 3]);
    SCHED();
  }
  put(8, cc);
  get(xf);
#pragma unroll
  for (int b = 0; b < 4; ++b) {
    e[1] = cmul(e[1], wb); e[3] = cmul(e[3], wb);
    put(2 * b, e[1]); put(2 * b + 1, e[3]);
  }
  const u32 d32 = pk(k0[3], k0[2]);
  {
    const u32 d10 = pk(k0[1], k0[0]), d21 = pk(k0[2], k0[1]);
    w64(e0 + TPE + 1080, u32x2{d32, d10});
    w16(e0 + TPO + 1082, d32); w32(e0 + TPO + 1084, d21); w16(e0 + TPO + 1088, d10 >> 16);
  }
  SCHED();
#pragma unroll
  for (int n = 0; n < 8; ++n) {
    k1 = mfma(xf[n], yf[n], k1);
    e[3] = cmul(e[3], wb); put(8 + n, e[3]);
    SCHED();
  }
  get(xf);
  {
    const u32 c33 = pk(k1[3], k1[3]), c32 = pk(k1[3], k1[2]), c11 = pk(k1[1], k1[1]), c10 = pk(k1[1], k1[0]), c21 = pk(k1[2], k1[1]);
    if (lane < 32) {
      w128(e1 + TPO + 560, u32x4{c33, c32, c11, c10});
      w16(e1 + TPE + 558, c33); w32(e1 + TPE + 560, c33); w32(e1 + TPE + 564, c21); w32(e1 + TPE + 568, c11); w16(e1 + TPE + 572, c10 >> 16);
    }
    if (lane == 31 || lane == 32 || lane == 63) {
      const u32 v = lane == 31 ? c33 : lane == 63 ? d32 : pk(k1[0] + dsk, 0.f);
      w16(e2 + TPE, v); w16(e2 + TPO + 2, v);
    }
  }
  SCHED();
#pragma unroll
  for (int n = 0; n < 4; ++n) { k2 = mfma(xf[n], yf[n], k2); k2b = mfma(xf[4 + n], yf[4 + n], k2b); }
  STAMP(3);
#pragma unroll
  for (int c = 0; c < 4; ++c) {
    w64(L + (2 * c) * UB + 512 + tid * 8, u32x2{__builtin_amdgcn_perm(ur[1][c], ur[0][c], 0x05040100), __builtin_amdgcn_perm(ur[3][c], ur[2][c], 0x05040100)});
    w64(L + (2 * c + 1) * UB + 512 + tid * 8, u32x2{__builtin_amdgcn_perm(ur[1][c], ur[0][c], 0x07060302), __builtin_amdgcn_perm(ur[3][c], ur[2][c], 0x07060302)});
  }
  k2 += k2b;
  {
    const u32 f01 = pk(k2[0], k2[1]), f23 = pk(k2[2], k2[3]);
#pragma unroll
    for (int j = 0; j < 4; ++j) { const u32 v = (j < 2 ? f01 : f23) >> (16 * (j & 1)); w16(fw[j], v); w16(fw[j] + FCP + 2, v); }
  }
  const u32 k1024 = __builtin_amdgcn_readlane(pk(k2[3], k2[3]), 31);
  STAMP(4);
  WAITL0();
  BAR();
  STAMP(6);
  u32x4 U[2][8], Fa[2], Fb[2];
  auto rdn = [&](int d0, int n) {
    const int q = d0 & 1;
    if (n == 0) { Fa[q][0] = r32(ta + 1024 - d0 * 64); Fa[q][1] = r32(ta + 1028 - d0 * 64); }
    else if (n == 1) { Fa[q][2] = r32(ta + 1032 - d0 * 64); Fa[q][3] = r32(ta + 1036 - d0 * 64); }
    else if (n < 10) U[q][n - 2] = r128(ua + 480 + (n - 2) * 512 - d0 * 64);
    else if (n == 10) { Fb[q][0] = r32(ta + 512 - d0 * 64); Fb[q][1] = r32(ta + 516 - d0 * 64); }
    else { Fb[q][2] = r32(ta + 520 - d0 * 64); Fb[q][3] = r32(ta + 524 - d0 * 64); }
  };
#pragma unroll
  for (int n = 0; n < 12; ++n) rdn(7, n);
  SCHED();
  u32x4 T[10], BX[5], F32, W1[6];
  auto rdf = [&](int n) {
    if (n < 20) {
      const int k = n >> 1, hf = n & 1;
      const int o = k < 3 ? hf * F2S + 64 - 32 * k : k < 6 ? (2 + hf) * F2S + 64 - 32 * (k - 3) : 4 * F2S + hf * F3S + 96 - 32 * (k - 6);
      T[k][2 * hf] = r32(t2 + o); T[k][2 * hf + 1] = r32(t2 + o + 4);
    } else if (n < 22) { F32[2 * (n - 20)] = r32(ta + 8 * (n - 20)); F32[2 * (n - 20) + 1] = r32(ta + 8 * (n - 20) + 4); }
    else if (n < 27) BX[n - 22] = r128(L + BOX + lane * 16 + (n - 22) * 1024);
    else if (n < 33) W1[n - 27] = r128(ua + 464 + (n - 27) * 512);
  };
  f32x4 acc[8];
#pragma unroll
  for (int d0 = 7; d0 >= 0; --d0) {
    const int q = d0 & 1;
#pragma unroll
    for (int m = 0; m < 15; ++m) {
      if (d0 < 7 && m == 0) __builtin_amdgcn_s_waitcnt(0xC97F);
      if (d0 < 7 && m == 4) __builtin_amdgcn_s_waitcnt(0xC77F);
      if (m >= 8) acc[m - 7] = mfma(Fb[q], U[q][m - 8], acc[m - 7]);
      else acc[m] = mfma(Fa[q], U[q][m], d0 == 7 ? zero : acc[m]);
      SCHED();
      if (d0 > 0) { if (m < 12) rdn(d0 - 1, m); else rdf((7 - d0) * 3 + m - 12); }
      else rdf(21 + m);
      SCHED();
    }
  }
  STAMP(7);
  {
    const u32x4 FX = BX[4] & k1024;
    f32x4 s[6];
    u32x4 SA, SB, SC;
    auto box = [&](int b) {
      s[0] = mfma(U[0][b], BX[0], zero); s[1] = mfma(U[0][b], BX[1], zero);
      s[2] = mfma(W1[b], BX[0], zero); s[3] = mfma(W1[b], BX[1], zero);
      if (b < 4) { s[4] = mfma(U[0][b], BX[2], zero); s[5] = mfma(W1[b], BX[3], zero); }
    };
    box(0);
#pragma unroll
    for (int b = 0; b < 6; ++b) {
      acc[b + 2] = mfma(F32, U[0][b], acc[b + 2]);
      if (b < 4) acc[b + 4] = mfma(FX, U[0][b], acc[b + 4]);
      SA = pack2(s[0], s[1]); SB = pack2(s[2], s[3]);
      if (b < 4) SC = pack2(s[4], s[5]);
      if (b < 5) box(b + 1);
#pragma unroll
      for (int d = 0; d < 3; ++d)
        if (b + 2 + d < 8) acc[b + 2 + d] = mfma(SA, T[d], acc[b + 2 + d]);
#pragma unroll
      for (int d = 0; d < 3; ++d)
        if (b + 2 + d < 8) acc[b + 2 + d] = mfma(SB, T[3 + d], acc[b + 2 + d]);
      if (b < 4) {
#pragma unroll
        for (int d = 0; d < 4; ++d)
          if (b + 4 + d < 8) acc[b + 4 + d] = mfma(SC, T[6 + d], acc[b + 4 + d]);
      }
    }
#pragma unroll
    for (int i = 0; i < 76; ++i) { __builtin_amdgcn_sched_group_barrier(0x8, 1, 0); __builtin_amdgcn_sched_group_barrier(0x2, 1, 0); }
  }
  STAMP(8);
  const rsrc_t rs = wsrsrc(ws);
  const u32 dst = WS_BT + chn * 4096 + (fh & 1) * 512 + (fr >> 2) * 128 + (fh >> 1) * 64 + (fr & 3) * 16;
#pragma unroll
  for (int i = 0; i < 8; i += 2) {
    auto sx = __builtin_amdgcn_permlane16_swap(pk(acc[i][0], acc[i][1]), pk(acc[i + 1][0], acc[i + 1][1]), false, false);
    auto sy = __builtin_amdgcn_permlane16_swap(pk(acc[i][2], acc[i][3]), pk(acc[i + 1][2], acc[i + 1][3]), false, false);
    st_sc1(rs, dst + i * 512, u32x4{sx[0], sy[0], sx[1], sy[1]});
  }
}

#define CODE_KB 40
__global__ __launch_bounds__(512, 2) void k1(const char* x, const char* w, const char* wo, const u32* logA, const u32* Aim,
                                             const u32* Cre, const u32* Cim, const u16* logdt, const u16* Dsk, const char* gw, char* ws) {
  extern __shared__ __attribute__((aligned(16))) char smem[];
  lp L = (lp)smem;
  STDECL;
  STAMP(0);
  const int xcd = blockIdx.x & 7, loc = blockIdx.x >> 3;
  const int wave = __builtin_amdgcn_readfirstlane(threadIdx.x >> 6), lane = threadIdx.x & 63;
  const bool isq = loc >= 25;
  const int Q = xcd >> 1, l = loc - 25;
  const int rq = ((0x8530 >> (4 * Q)) & 15) + (l >> 1), rk = Q * 4 + (loc >> 2);
  const int cq = (xcd & 1) * 2 + (l & 1), ck = (xcd & 1) * 4 + (loc & 3);
  const int sq = 3 * (l >> 1) + 4 * (l & 1) + 1, sk = 2 * ((loc >> 2) + (loc & 3));
  int rbi = isq ? rq : rk, cg = isq ? cq : ck, c0 = isq ? sq : sk;
  c0 = c0 >= 9 ? c0 - 9 : c0;
  asm volatile("" : "+s"(rbi), "+s"(cg), "+s"(c0));
  PINS(x); PINS(w); PINS(wo); PINS(logA); PINS(Aim); PINS(Cre); PINS(Cim); PINS(logdt); PINS(Dsk); PINS(gw); PINS(ws);
  if (Q == 1 && l >= 4) return;
  if (__builtin_expect((loc < 16 || isq) && wave < 4, 1)) {
    role_load(L, x, w, wo, gw, ws, isq, rbi, cg, c0, wave, lane STPASS);
  } else if (loc < 16) {
    role_kv(L, ws, rbi, cg, wave - 4, lane STPASS);
  } else if (isq) {
    role_q(L, ws, rbi, cg, wave - 4, lane STPASS);
  } else {
    role_ssm(L, x, logA, Aim, Cre, Cim, logdt, Dsk, ws, xcd * 9 + loc - 16, wave, lane STPASS);
  }
  STFLUSH(0);
}

__global__ __launch_bounds__(576) void k2(const char* ws, char* out) {
  extern __shared__ __attribute__((aligned(16))) char smem[];
  lp L = (lp)smem;
  STDECL;
  STAMP(0);
  const int xcd = blockIdx.x & 7, id = xcd * 29 + (xcd < 2 ? xcd : 2) + (blockIdx.x >> 3);
  const int rb = (id * 7282) >> 16, cb = id - rb * 9;
  const int wave = __builtin_amdgcn_readfirstlane(threadIdx.x >> 6), lane = threadIdx.x & 63;
  int rot = (cb + 2 * rb) & 7; rot = rot >= 5 ? rot - 5 : rot;
  constexpr int BL = 73728;
  auto grp = [&](int sl) { int gg = sl - 1 + rot; return sl == 0 ? 5 : gg >= 5 ? gg - 5 : gg; };
  if (wave < 4) {
    const char* mb = ws + WS_M + cb * 73728 + wave * 3072;
    const u32 lo = lane * 16;
    auto issue = [&](int sl) { glds3(mb + grp(sl) * 12288, sl * 12288 + wave * 3072, lo); };
    issue(0); issue(1); issue(2);
    u32 bo[3];
#pragma unroll
    for (int i = 0; i < 3; ++i) {
      const int f = wave * 160 + i * 64 + (i < 2 ? lane : lane & 31);
      const int ir = (f * 6554) >> 16, pc = f - ir * 10, hh = pc >= 5;
      int rt = 5 * rb + pc - 5 * hh; rt = rt > 127 ? 127 : rt;
      bo[i] = (4 * (ir & 15) + (ir >> 4)) * 4096 + (rt >> 2) * 128 + hh * 64 + (rt & 3) * 16;
    }
    waitv<6>(); BAR();
    issue(3);
    {
      const char* bt = ws + WS_BT + cb * (64 * 4096);
      glds(bt, bo[0], BL + wave * 2560); glds(bt, bo[1], BL + wave * 2560 + 1024);
      glds_lo(bt, bo[2], BL + wave * 2560 + 2048);
    }
    waitv<9>(); BAR();
    issue(4); waitv<9>(); BAR();
    issue(5); waitv<9>(); BAR();
    waitv<3>(); BAR();
    waitv<0>(); BAR();
    STFLUSH(1);
    return;
  }
  const int cw = wave - 4, fr = lane & 15, fh = lane >> 4;
  int rt = 5 * rb + cw;
  const bool live = rt < 128;
  rt = live ? rt : 127;
  const char* pq = ws + WS_PQ + rt * (18 * 1024);
  u32x4 a[18];
  auto lda = [&](int sl) {
#pragma unroll
    for (int s = 0; s < 3; ++s) {
      if (sl == 0 && s > 0) {
        const char* pe = pq + (15 + s) * 1024 + (lane & 31) * 16 + (lane >> 5) * 8;
        const u32x2 lo = *(const u32x2*)pe, hi = *(const u32x2*)(pe + 512);
        a[s] = u32x4{lo[0], lo[1], hi[0], hi[1]};
      } else {
        a[sl * 3 + s] = *(const u32x4*)(pq + (grp(sl) * 3 + s) * 1024 + lane * 16);
      }
    }
  };
  lda(0); lda(1); lda(2);
  const f32x4 g = *(const f32x4*)(ws + WS_G + (rt * 16 + fh * 4) * 4);
  f32x4 acc[4], cst[4];
  lp B[4];
#pragma unroll
  for (int t = 0; t < 4; ++t) { acc[t] = f32x4{0.f, 0.f, 0.f, 0.f}; B[t] = L + fh * 1024 + fr * 64 + ((t ^ (fr >> 2)) << 4); PIN(B[t]); }
  u32x4 b[2][12];
#pragma unroll
  for (int sl = 0; sl < 7; ++sl) {
    if (sl < 6) { BAR(); STAMP(1 + sl); }
    if (sl < 3) lda(sl + 3);
    SCHED();
#pragma unroll
    for (int n = 0; n < 12; ++n) {
      const int s = n >> 2, t = n & 3;
      if (sl < 6) b[sl & 1][n] = (sl == 0 && s > 0) ? r128(L + lane * 16 + n * 1024) : r128(B[t] + sl * 12288 + s * 4096);
      SCHED();
      if (sl > 0) {
        const int q = (sl - 1) & 1;
        const u32x4 bf = (sl == 1 && s > 0) ? u32x4{b[q][4 * s][t], b[q][4 * s + 1][t], b[q][4 * s + 2][t], b[q][4 * s + 3][t]} : b[q][n];
        acc[t] = mfma(a[(sl - 1) * 3 + s], bf, acc[t]);
      }
      SCHED();
    }
    if (sl == 4) {
#pragma unroll
      for (int t = 0; t < 4; ++t) {
        const u32x2 bv = r64(L + BL + (16 * t + fr) * 160 + ((fh >> 1) * 5 + cw) * 16 + (fh & 1) * 8);
        cst[t] = f32x4{bf2f(bv[0]), bfhi(bv[0]), bf2f(bv[1]), bfhi(bv[1])} * (1.f - g);
        PIN(cst[t]);
      }
    }
  }
  STAMP(7);
  const rsrc_t ro = __builtin_amdgcn_make_buffer_rsrc(out, 0, SEQ * XROW, 0x00020000);
  const u32 dst = live ? (rt * 16 + fh * 4) * XROW + cb * 128 + fr * 8 : OOR;
#pragma unroll
  for (int t = 0; t < 4; ++t) acc[t] = acc[t] * g + cst[t];
#pragma unroll
  for (int j = 0; j < 4; ++j)
    __builtin_amdgcn_raw_buffer_store_b64(u32x2{pk(acc[0][j], acc[1][j]), pk(acc[2][j], acc[3][j])}, ro, dst + j * XROW, 0, AUX_OUT);
  STFLUSH(1);
}

extern "C" void kernel_launch(void* const* d_in, const int* in_sizes, int n_in, void* d_out, int out_size, void* d_ws,
                              size_t ws_size, hipStream_t stream) {
  (void)hipFuncSetAttribute((const void*)k1, hipFuncAttributeMaxDynamicSharedMemorySize, K1_LDS);
  (void)hipFuncSetAttribute((const void*)k2, hipFuncAttributeMaxDynamicSharedMemorySize, K2_LDS);
  k1<<<248, 512, K1_LDS, stream>>>((const char*)d_in[0], (const char*)d_in[1], (const char*)d_in[2], (const u32*)d_in[4],
                                   (const u32*)d_in[5], (const u32*)d_in[6], (const u32*)d_in[7], (const u16*)d_in[8],
                                   (const u16*)d_in[9], (const char*)d_in[10], (char*)d_ws);
  k2<<<234, 576, K2_LDS, stream>>>((const char*)d_ws, (char*)d_out);
}
```

```cpp
#include <hip/hip_runtime.h>
#include <stdint.h>

typedef unsigned int u32;
typedef unsigned short u16;
typedef unsigned long long u64;
typedef __attribute__((ext_vector_type(2))) unsigned u32x2;
typedef __attribute__((ext_vector_type(4))) unsigned u32x4;
typedef __attribute__((ext_vector_type(2))) float f32x2;
typedef __attribute__((ext_vector_type(4))) float f32x4;
typedef __attribute__((ext_vector_type(4))) short s16x4;
typedef __attribute__((ext_vector_type(2))) __bf16 bf16x2;
typedef __attribute__((ext_vector_type(8))) __bf16 bf16x8;
#define LAS __attribute__((address_space(3)))
#define GAS __attribute__((address_space(1)))
#define DI __device__ __forceinline__
typedef LAS char* lp;

constexpr int SEQ = 2048, DM = 576;
constexpr int XROW = DM * 2;
constexpr int WROW = 3 * DM * 2;
constexpr int WS_PQ = 0;
constexpr int WS_M = WS_PQ + 128 * 18 * 1024;
constexpr int WS_BT = WS_M + 36 * 18 * 1024;
constexpr int WS_G = WS_BT + DM * SEQ * 2;
constexpr int WS_MBKV = WS_G + SEQ * 4;
constexpr int MBKV_SZ = 16 * 36 * 16;
constexpr int WS_MBZ = WS_MBKV + 128 * MBKV_SZ;
constexpr int MBZ_SZ = 2 * 16 * 20 * 16;
constexpr int WS_EP = WS_MBZ + 44 * MBZ_SZ;
constexpr int WS_END = WS_EP + 176 * 64;
constexpr int K1_LDS = 162304;
constexpr int K2_LDS = 5 * 27648 + 10240 + 320;
#define PF_BLOCKS 1
#define AUX_OUT 16

DI float bf2f(u32 v) { return __uint_as_float(v << 16); }
DI float bfhi(u32 v) { return __uint_as_float(v & 0xffff0000u); }
DI u32 pk(float a, float b) { bf16x2 v = {(__bf16)a, (__bf16)b}; return __builtin_bit_cast(u32, v); }
DI float rbf(float a) { return bf2f(pk(a, a) & 0xffffu); }
DI f32x4 mfma(u32x4 a, u32x4 b, f32x4 c) {
  return __builtin_amdgcn_mfma_f32_16x16x32_bf16(__builtin_bit_cast(bf16x8, a), __builtin_bit_cast(bf16x8, b), c, 0, 0, 0);
}
DI u32x4 r128(lp a) { return *(LAS u32x4*)a; }
DI u32x2 r64(lp a) { return *(LAS u32x2*)a; }
DI u32 r32(lp a) { return *(LAS u32*)a; }
DI void w128(lp a, u32x4 v) { *(LAS u32x4*)a = v; }
DI void w64(lp a, u32x2 v) { *(LAS u32x2*)a = v; }
DI void w32(lp a, u32 v) { *(LAS u32*)a = v; }
DI void w16(lp a, u32 v) { *(LAS u16*)a = (u16)v; }
DI u32x2 rtr(lp a) { return __builtin_bit_cast(u32x2, __builtin_amdgcn_ds_read_tr16_b64_v4i16((LAS s16x4*)a)); }
DI u32x4 rtr2(lp a0, lp a1) { u32x2 l = rtr(a0), h = rtr(a1); return u32x4{l[0], l[1], h[0], h[1]}; }
DI u32x4 r4x32(lp a) { return u32x4{r32(a), r32(a + 4), r32(a + 8), r32(a + 12)}; }
DI u32x4 r2x2(lp a, lp b) { return u32x4{r32(a), r32(a + 4), r32(b), r32(b + 4)}; }
DI void glds(const void* sbase, u32 voff, u32 lds) {
  asm volatile("s_mov_b32 m0, %2\n\ts_nop 0\n\tglobal_load_lds_dwordx4 %0, %1" ::"v"(voff), "s"(sbase), "s"(lds) : "memory");
}
DI void glds4(const void* sbase, u32 lds, u32 v0, u32 v1, u32 v2, u32 v3) {
  asm volatile("s_mov_b32 m0, %5\n\ts_nop 0\n\tglobal_load_lds_dwordx4 %0, %4\n\tglobal_load_lds_dwordx4 %1, %4 offset:1024\n\t"
               "global_load_lds_dwordx4 %2, %4 offset:2048\n\tglobal_load_lds_dwordx4 %3, %4 offset:3072"
               ::"v"(v0), "v"(v1), "v"(v2), "v"(v3), "s"(sbase), "s"(lds) : "memory");
}
DI void glds2(const void* sbase, u32 lds, u32 v0, u32 v1) {
  asm volatile("s_mov_b32 m0, %3\n\ts_nop 0\n\tglobal_load_lds_dwordx4 %0, %2\n\tglobal_load_lds_dwordx4 %1, %2 offset:1024"
               ::"v"(v0), "v"(v1), "s"(sbase), "s"(lds) : "memory");
}
DI void glds3(const void* sbase, u32 lds, u32 v) {
  asm volatile("s_mov_b32 m0, %2\n\ts_nop 0\n\tglobal_load_lds_dwordx4 %0, %1\n\tglobal_load_lds_dwordx4 %0, %1 offset:1024\n\t"
               "global_load_lds_dwordx4 %0, %1 offset:2048" ::"v"(v), "s"(sbase), "s"(lds) : "memory");
}
DI void glds_lo(const void* sbase, u32 voff, u32 lds) {
  asm volatile("s_mov_b32 m0, %2\n\ts_mov_b32 exec_hi, 0\n\tglobal_load_lds_dwordx4 %0, %1\n\ts_mov_b32 exec_hi, -1"
               ::"v"(voff), "s"(sbase), "s"(lds) : "memory");
}
template <int N> DI void waitv() { asm volatile("s_waitcnt vmcnt(%0)" ::"i"(N) : "memory"); }
#define CFENCE() asm volatile("" ::: "memory")
#define WAITL0() do { __builtin_amdgcn_s_waitcnt(0xC07F); CFENCE(); } while (0)
#define BAR() do { CFENCE(); __builtin_amdgcn_s_barrier(); CFENCE(); } while (0)
#define SCHED() __builtin_amdgcn_sched_barrier(0)
#define PIN(x) asm volatile("" : "+v"(x))
#define PINS(x) asm volatile("" ::"s"(x))
constexpr float EPS = 1e-6f, L2E = 1.44269504089f;
DI f32x2 phi2(float a0, float a1) {
  f32x2 m = f32x2{a0, a1} * L2E - EPS * L2E;
  f32x2 e = {__builtin_amdgcn_fmed3f(__builtin_amdgcn_exp2f(m[0]), 0.f, 1.f), __builtin_amdgcn_fmed3f(__builtin_amdgcn_exp2f(m[1]), 0.f, 1.f)};
  const int ei = __builtin_bit_cast(int, EPS);
  f32x2 x = {__int_as_float(max(__float_as_int(a0), ei)), __int_as_float(max(__float_as_int(a1), ei))};
  return x + e;
}
DI f32x4 phi4(f32x4 a) { f32x2 l = phi2(a[0], a[1]), h = phi2(a[2], a[3]); return f32x4{l[0], l[1], h[0], h[1]}; }
DI float rowsum16(float v) {
  v += __builtin_bit_cast(float, __builtin_amdgcn_update_dpp(0, __builtin_bit_cast(int, v), 0x128, 0xf, 0xf, false));
  v += __builtin_bit_cast(float, __builtin_amdgcn_update_dpp(0, __builtin_bit_cast(int, v), 0x124, 0xf, 0xf, false));
  v += __builtin_bit_cast(float, __builtin_amdgcn_update_dpp(0, __builtin_bit_cast(int, v), 0x122, 0xf, 0xf, false));
  v += __builtin_bit_cast(float, __builtin_amdgcn_update_dpp(0, __builtin_bit_cast(int, v), 0x121, 0xf, 0xf, false));
  return v;
}
DI float prodsum(float a, float b, float c, float d) {
  auto s0 = __builtin_amdgcn_permlane32_swap(__float_as_uint(a), __float_as_uint(c), false, false);
  auto s1 = __builtin_amdgcn_permlane32_swap(__float_as_uint(b), __float_as_uint(d), false, false);
  const float ac = __uint_as_float(s0[0]) + __uint_as_float(s0[1]), bd = __uint_as_float(s1[0]) + __uint_as_float(s1[1]);
  auto s2 = __builtin_amdgcn_permlane16_swap(__float_as_uint(ac), __float_as_uint(bd), false, false);
  float v = __uint_as_float(s2[0]) + __uint_as_float(s2[1]);
  v += __builtin_bit_cast(float, __builtin_amdgcn_update_dpp(0, __builtin_bit_cast(int, v), 0x128, 0xf, 0xf, false));
  v += __builtin_bit_cast(float, __builtin_amdgcn_update_dpp(0, __builtin_bit_cast(int, v), 0x124, 0xf, 0xf, false));
  return v;
}
DI float prodsum(u32x4 m) { return prodsum(bf2f(m[0]), bfhi(m[0]), bf2f(m[2]), bfhi(m[2])); }
DI u32x4 pack2(f32x4 a, f32x4 b) { return u32x4{pk(a[0], a[1]), pk(a[2], a[3]), pk(b[0], b[1]), pk(b[2], b[3])}; }
typedef __amdgpu_buffer_rsrc_t rsrc_t;
DI rsrc_t wsrsrc(char* ws) { return __builtin_amdgcn_make_buffer_rsrc(ws, 0, WS_END, 0x00020000); }
DI void st_sc1(rsrc_t r, u32 off, u32x4 v) { __builtin_amdgcn_raw_buffer_store_b128(v, r, off, 0, 16); }
DI u32x4 ld_sc1(rsrc_t r, u32 off) { return __builtin_amdgcn_raw_buffer_load_b128(r, off, 0, 16); }
DI void st32_sc1(rsrc_t r, u32 off, u32 v) { __builtin_amdgcn_raw_buffer_store_b32(v, r, off, 0, 16); }
DI u32 ld_ep(char* ws, int i) { return __hip_atomic_load((GAS u32*)(ws + WS_EP) + 16 * i, __ATOMIC_RELAXED, __HIP_MEMORY_SCOPE_AGENT); }
DI void st_ep(rsrc_t r, int i, int lane, u32 v) { if (lane < 4) st_sc1(r, WS_EP + 64 * i + 16 * lane, u32x4{v, v, v, v}); }
constexpr u32 OOR = 0x7ffffff0u;
constexpr u32 SPIN_CAP = 1u << 15;
#define POLL_SLEEP 4
#define STDECL
#define STARG
#define STPASS
#define STAMP(i)
#define STFLUSH(k)

constexpr int XR_KV = 4 * 18432, XR_Q = 3 * 18432;
constexpr int GWL = XR_KV + 4 * 16384;
constexpr int WOI = GWL + 1280;
constexpr int XCH = XR_KV + 16384, MSG = 18432, KVA = MSG + 4096;

template <int ROLE>
DI void gemm_main(lp L, int rg, int lane, f32x4 (&acc)[ROLE ? 3 : 2][9] STARG) {
  constexpr int RT = ROLE ? 3 : 2, XB = RT * 8192, XRING = ROLE ? XR_Q : XR_KV, NS = ROLE ? 3 : 4;
  const int fr = lane & 15, fh = lane >> 4;
  const int q4 = (lane >> 2) & 3, p4 = lane & 3;
  const int ax0 = XRING + (rg * RT * 16 + fr) * 128 + ((fh ^ ((fr >> 1) & 7)) << 4), ax1 = ax0 ^ 64;
  const int r0 = (8 * fh + q4 + 4 * (fh & 1)) * 288, r1 = (8 * fh + q4 + 4 * ((fh & 1) ^ 1)) * 288;
  const int cm = ROLE ? 72 * p4 : 8 * p4, cx = 72 * p4 + 32;
  u32x4 a0[RT], a1[RT], b0[9], b1[9];
  lp W0 = L + r0 + cm, W1 = L + r1 + cm, E0 = L + r0 + cx, E1 = L + r1 + cx, X0 = L + ax0, X1 = L + ax1;
  PIN(E0); PIN(E1);
  auto step = [&](bool domma, u32x4 (&a)[RT], u32x4 (&b)[9], int slot, int s, u32x4 (&an)[RT], u32x4 (&bn)[9]) {
    auto rd = [&](int n) {
      if (n < RT) { an[n] = r128((s ? X1 : X0) + slot * XB + n * 2048); return; }
      n -= RT;
      if (n >= 18) return;
      const int t = n >> 1, so = slot * 18432 + s * 9216;
      const int to = (ROLE ? 8 * t : 72 * (t >> 1) + 40 * (t & 1)) + so;
      const u32x2 v = (ROLE || t < 8) ? rtr((n & 1 ? W1 : W0) + to) : rtr((n & 1 ? E1 : E0) + so);
      bn[t][2 * (n & 1)] = v[0]; bn[t][2 * (n & 1) + 1] = v[1];
    };
    const int EX = ROLE ? 0 : (s ? 7 : 2);
#pragma unroll
    for (int m = 0; m < 9 * RT; ++m) {
      const int t = m / RT, i = m % RT;
      if (domma) acc[i][t] = ROLE ? mfma(b[t], a[i], acc[i][t]) : mfma(a[i], b[t], acc[i][t]);
      SCHED();
      if (m < 2 * EX) { rd(m + (m + 1) / 2); if (!(m & 1)) rd(m + m / 2 + 1); } else rd(m + EX);
      SCHED();
    }
  };
#pragma unroll
  for (int i = 0; i < RT; ++i)
#pragma unroll
    for (int t = 0; t < 9; ++t) { const float v = (ROLE || t < 4 || (t == 8 && fr < 8)) ? EPS : 0.f; acc[i][t] = f32x4{v, v, v, v}; PIN(acc[i][t]); }
  STAMP(1);
  BAR();
  STAMP(2);
  step(false, a1, b1, 0, 0, a0, b0);
#pragma unroll 1
  for (int it = 0; it < (ROLE ? 3 : 2); ++it) {
#pragma unroll
    for (int sl = 0; sl < NS; ++sl) {
      step(true, a0, b0, sl, 1, a1, b1);
      WAITL0();
      BAR();
      SCHED();
      step(true, a1, b1, sl == NS - 1 ? 0 : sl + 1, 0, a0, b0);
    }
  }
  if (ROLE == 0) {
    step(true, a0, b0, 0, 1, a1, b1);
#pragma unroll
    for (int t = 0; t < 9; ++t)
#pragma unroll
      for (int i = 0; i < RT; ++i) acc[i][(t + 8) % 9] = mfma(a1[i], b1[(t + 8) % 9], acc[i][(t + 8) % 9]);
  }
}

DI void kv_consume(lp L, rsrc_t rs, int rb, int g, u32 tme, int wave, int lane STARG) {
  const int chead = 4 * g + (rb & 3), crole = rb >> 2;
  if (crole == 3) return;
  const int fr = lane & 15, fh = lane >> 4;
  const bool ext = crole == 2;
  u32x4 wf[6];
  {
    const int codd = chead & 1, q4 = (lane >> 2) & 3, p4 = lane & 3;
    int s0 = 8 * fh + q4, s1 = s0 + 4;
    s0 = s0 > 17 ? 17 : s0; s1 = s1 > 17 ? 17 : s1;
    s0 += 2 * codd; s1 += 2 * codd;
    s0 = s0 >= 18 ? s0 - 18 : s0; s1 = s1 >= 18 ? s1 - 18 : s1;
    lp p0 = L + WOI + s0 * XROW + wave * 64 + p4 * 8, p1 = L + WOI + s1 * XROW + wave * 64 + p4 * 8;
#pragma unroll
    for (int i = 0; i < 6; ++i) wf[i] = rtr2(p0 + (i >> 1) * 512 + (i & 1) * 32, p1 + (i >> 1) * 512 + (i & 1) * 32);
  }
  const int un = 2 * (chead >> 3) + (chead & 1), hh = (chead >> 1) & 3;
  const int col = (wave & 1) * 32 + (lane & 31);
  u32 mo = WS_M + (wave >> 1) * 73728 + (ext ? 65536 + (un * 4 + hh) * 256 + col * 4 : (2 * un + crole) * 4096 + hh * 1024 + ((col ^ (col >> 4)) << 4));
  if (lane >= 32) mo = OOR;
  PIN(mo);
  STAMP(6);
  if (wave < (ext ? 3 : 8)) {
    const int it = 4 * wave + (lane & 3);
    const u32 off = WS_MBKV + (g * 16 + rb) * MBKV_SZ + (lane >> 2) * 576 + it * 16;
    const bool two = wave == 0 && !ext;
    lp d = L + KVA + (ext ? (fh & 1) * 64 + it * 4 + (fh >> 1) * 2 : ((it & 1) * 4 + fh) * 64 + (it >> 1) * 2);
    PIN(d);
    u32x4 m, e = {0, tme, 0, tme};
    for (u32 spin = 0;; ++spin) {
      m = ld_sc1(rs, off);
      if (two) e = ld_sc1(rs, off + 512);
      bool ok = m[1] == tme && m[3] == tme && e[1] == tme && e[3] == tme;
      if (__all(ok) || spin > SPIN_CAP) break;
      __builtin_amdgcn_s_sleep(POLL_SLEEP);
    }
    STAMP(7);
    const float sm = prodsum(m);
    if (fr < 4 && !(ext && it > 8)) w16(d, pk(sm, sm));
    if (two) { const float se = prodsum(e); if (fr < 4) w16(d + 32, pk(se, se)); }
  }
  WAITL0();
  BAR();
  STAMP(8);
  const u32x4 af = r128(L + KVA + fr * 64 + fh * 16);
#pragma unroll
  for (int i = 0; i < 3; ++i) {
    if (i == 2 && wave >= 2) break;
    const f32x4 u = mfma(af, wf[2 * i], f32x4{0.f, 0.f, 0.f, 0.f}), v = mfma(af, wf[2 * i + 1], f32x4{0.f, 0.f, 0.f, 0.f});
    auto sx = __builtin_amdgcn_permlane16_swap(pk(u[0], u[1]), pk(v[0], v[1]), false, false);
    auto sy = __builtin_amdgcn_permlane16_swap(pk(u[2], u[3]), pk(v[2], v[3]), false, false);
    if (ext) st32_sc1(rs, mo + i * (4 * 73728), sx[0]);
    else st_sc1(rs, mo + i * (4 * 73728), u32x4{sx[0], sy[0], sx[1], sy[1]});
  }
  if (wave == 7) st_ep(rs, g * 16 + rb, lane, tme);
}

DI void role_load(lp L, const char* x, const char* w, const char* wo_g, char* ws, bool isq, int rbi, int cg, int c0,
                  int lw, int lane STARG) {
  __builtin_amdgcn_s_setprio(2);
  const bool six = isq && rbi < 10;
  const int XP = isq ? 6 : 4;
  const char* xr = x + rbi * (isq ? 192 * XROW : 128 * XROW);
  const u32 xl = (isq ? XR_Q : XR_KV) + lw * XP * 1024, XB = XP * 4096;
  u32 xo[4], wo[5];
  xo[0] = (8 * (six || !isq ? lw * XP : (lw * XP > 12 ? 12 : lw * XP)) + (lane >> 3)) * XROW + (((lane & 7) ^ (lane >> 4)) << 4);
  xo[1] = (xo[0] ^ 64) + 8192; xo[2] = xo[0] + 16384; xo[3] = xo[1] + 16384;
  auto issx = [&](int cidx, int slot) {
    const char* xb = xr + cidx * 128;
    glds4(xb, xl + slot * XB, xo[0], xo[1], xo[2], xo[3]);
    if (isq) glds2(xb + (six || lw < 2 ? 32 * XROW : 0), xl + slot * XB + 4096, xo[0], xo[1]);
  };
  auto issw = [&](int cidx, int slot) {
    const char* wb = w + cidx * (64 * WROW);
    const u32 lb = slot * 18432 + lw * 4608;
    glds4(wb, lb, wo[0], wo[1], wo[2], wo[3]);
    glds_lo(wb, wo[4], lb + 4096);
  };
  auto nxt = [](int c) { return c == 8 ? 0 : c + 1; };
  STAMP(3);
  issx(c0, 0);
  SCHED();
  {
    const int cb0 = (isq ? 288 * cg : 1152 + 144 * cg) + 16 * lw * WROW, cb1 = isq ? cb0 : cb0 + 1008;
#pragma unroll
    for (int i = 0; i < 5; ++i) {
      const int f = i < 4 ? 64 * i + lane : 256 + (lane & 31);
      const int lr = (f * 3641) >> 16, cc = f - lr * 18;
      wo[i] = (lr ^ ((lr >> 1) & 4)) * WROW + (cc < 9 ? cb0 : cb1) + 16 * cc - (i < 4 ? 1024 * i : 0);
    }
  }
  issw(c0, 0);
  STAMP(9);
  int c = nxt(c0);
  const int fr = lane & 15, fh = lane >> 4;
  const int me = isq ? 128 + cg * 11 + rbi : cg * 16 + rbi;
  const int jq = lane >> 2, cz = (cg >> 1) * 11 + (jq > 10 ? 10 : jq);
  u32 tme, tz;
  if (isq) {
    issx(c, 1); issw(c, 1);
    c = nxt(c);
    int sx = 2;
    STAMP(1);
    waitv<11>();
    BAR();
    STAMP(2);
#pragma unroll 1
    for (int k = 0; k < 7; ++k) {
      issx(c, sx); issw(c, sx);
      c = nxt(c);
      sx = sx == 2 ? 0 : sx + 1;
      waitv<11>();
      BAR();
    }
    waitv<0>();
    tme = ld_ep(ws, me); tz = 0;
    BAR();
  } else {
    issx(c, 1); issw(c, 1);
    c = nxt(c);
    const char* wb = w + c * (64 * WROW);
    const char* xb = xr + c * 128;
    asm volatile("" : "+s"(wb), "+s"(xb));
    STAMP(1);
    waitv<9>();
    BAR();
    STAMP(2);
    glds4(xb, xl + 2 * 16384, xo[0], xo[1], xo[2], xo[3]);
#pragma unroll
    for (int k = 0; k < 7; ++k) {
      const u32 lb = ((k + 2) & 3) * 18432 + lw * 4608;
      glds4(wb, lb, wo[0], wo[1], wo[2], wo[3]);
      glds_lo(wb, wo[4], lb + 4096);
      if (k < 6) {
        c = nxt(c);
        xb = xr + c * 128; wb = w + c * (64 * WROW);
        asm volatile("" : "+s"(wb), "+s"(xb));
        glds4(xb, xl + ((k + 3) & 3) * 16384, xo[0], xo[1], xo[2], xo[3]);
        waitv<13>();
      } else {
        waitv<9>();
      }
      BAR();
    }
    if (rbi < 12) {
      const char* src = wo_g + (4 * cg + (rbi & 3)) * (18 * XROW) + lw * 5184;
      glds4(src, WOI + lw * 5184, lane * 16, lane * 16, lane * 16, lane * 16);
      glds(src + 4096, lane * 16, WOI + lw * 5184 + 4096);
      if (lane < 4) glds(src + 5120, lane * 16, WOI + lw * 5184 + 5120);
      waitv<6>();
    } else {
      waitv<0>();
    }
    BAR();
    tme = ld_ep(ws, me); tz = ld_ep(ws, 128 + cz);
  }
  const rsrc_t rs = wsrsrc(ws);
  tme += 1; tz += 1;
  if (isq) {
    BAR();
    const int i0 = lw * 4 + (lane & 3);
    const u32 base = WS_MBZ + (me - 128) * MBZ_SZ + (lane >> 2) * 320;
    u32 o[3]; lp zw[3];
#pragma unroll
    for (int i = 0; i < 3; ++i) {
      const int it = i0 + 16 * i, gi = it >= 20, r = it - 20 * gi;
      o[i] = base + gi * (MBZ_SZ / 2) + r * 16;
      zw[i] = L + GWL + (r < 16 ? (2 * gi + (r >> 3)) * 160 + (r & 7) * 16 : (2 * gi + (r & 1)) * 160 + 128 + (r & 2) * 8) + fh * 4;
    }
    u32x4 m[3];
    m[2] = u32x4{0, tme, 0, tme};
    for (u32 spin = 0;; ++spin) {
      bool ok = true;
      m[0] = ld_sc1(rs, o[0]); m[1] = ld_sc1(rs, o[1]);
      if (lw < 2) m[2] = ld_sc1(rs, o[2]);
#pragma unroll
      for (int i = 0; i < 3; ++i) ok = ok && m[i][1] == tme && m[i][3] == tme;
      if (__all(ok) || spin > SPIN_CAP) break;
      __builtin_amdgcn_s_sleep(POLL_SLEEP);
    }
    STAMP(7);
#pragma unroll
    for (int i = 0; i < 3; ++i) {
      if (i == 2 && lw >= 2) break;
      const float sv = prodsum(m[i]);
      if (fr < 4) w32(zw[i], __float_as_uint(sv));
    }
    WAITL0();
    BAR();
    if (lw == 0) st_ep(rs, me, lane, tme);
    return;
  }
  __builtin_amdgcn_s_setprio(0);
  if (lw == 0) w128(L + KVA + lane * 16, u32x4{0, 0, 0, 0});
  u32 zo = jq < 11 ? WS_MBZ + cz * MBZ_SZ + (cg & 1) * (MBZ_SZ / 2) + rbi * 320 + lw * 64 + (lane & 3) * 16 : OOR;
  PIN(zo);
  waitv<0>();
  WAITL0();
  BAR();
  STAMP(4);
  {
    const u32x4 one = {0x3f803f80u, 0x3f803f80u, 0x3f803f80u, 0x3f803f80u};
#pragma unroll
    for (int k = 0; k < 2; ++k) {
      if (k && lw) break;
      lp bk = L + XCH + lane * 16 + (k ? 8192 : lw * 1024);
      u32x4 kf[4];
#pragma unroll
      for (int r = 0; r < 4; ++r) kf[r] = r128(bk + r * 9216);
      const f32x4 t = mfma(kf[1], one, mfma(kf[0], one, f32x4{0.f, 0.f, 0.f, 0.f})) + mfma(kf[3], one, mfma(kf[2], one, f32x4{0.f, 0.f, 0.f, 0.f}));
      const u32 p0 = __builtin_amdgcn_ds_bpermute((lane & 3) * 64, pk(t[0], t[1])), p1 = __builtin_amdgcn_ds_bpermute((lane & 3) * 64, pk(t[2], t[3]));
      st_sc1(rs, zo + k * 256, u32x4{p0, tz, p1, tz});
    }
  }
  STAMP(5);
  kv_consume(L, rs, rbi, cg, tme, lw, lane STPASS);
}

DI void role_kv(lp L, char* ws, int rb, int g, int hi, int lane STARG) {
  const int fr = lane & 15, fh = lane >> 4;
  const rsrc_t rs = wsrsrc(ws);
  const int c0 = g * 16 + hi + (lane < 36 ? 0 : 4), c1 = g * 16 + hi + (lane < 8 ? 4 : 8);
  u32 d0 = WS_MBKV + c0 * MBKV_SZ + rb * 576 + (lane < 36 ? lane : lane - 36) * 16;
  u32 d1 = lane < 20 ? WS_MBKV + c1 * MBKV_SZ + rb * 576 + (lane < 8 ? lane + 28 : lane - 8) * 16 : OOR;
  PIN(d0); PIN(d1);
  f32x4 acc[2][9];
  gemm_main<0>(L, hi, lane, acc STPASS);
  u32 t0 = ld_ep(ws, c0), t1 = ld_ep(ws, c1), tme = ld_ep(ws, g * 16 + rb);
  STAMP(3);
  {
    lp xw = L + XCH + hi * 9216 + lane * 16;
#pragma unroll
    for (int t = 0; t < 4; ++t) {
      w128(xw + t * 1024, pack2(phi4(acc[0][t]), phi4(acc[1][t])));
      w128(xw + 4096 + t * 1024, pack2(acc[0][4 + t], acc[1][4 + t]));
    }
    const u32x4 xp = pack2(phi4(acc[0][8]), phi4(acc[1][8])), xr = pack2(acc[0][8], acc[1][8]);
    w128(xw + 8192, fr < 8 ? xp : xr);
  }
  t0 += 1; t1 += 1; tme += 1;
  lp mb = L + MSG + hi * 1024;
  WAITL0();
  BAR();
  STAMP(4);
  {
    f32x4 p1 = {0.f, 0.f, 0.f, 0.f}, p2 = p1, p3 = p1, p4 = p1;
    lp base = L + XCH + lane * 16;
    u32x4 xf[4], vf[4], kf[4];
#pragma unroll
    for (int r = 0; r < 4; ++r) { kf[r] = r128(base + hi * 1024 + r * 9216); vf[r] = r128(base + 4096 + hi * 1024 + r * 9216); xf[r] = r128(base + r * 9216 + 8192); }
#pragma unroll
    for (int r = 0; r < 4; ++r) {
      p1 = mfma(kf[r], vf[r], p1); p2 = mfma(kf[r], xf[r], p2);
      p3 = mfma(xf[r], vf[r], p3); p4 = mfma(xf[r], xf[r], p4);
    }
    const bool own = (fr >> 1) == 4 + hi;
    lp w1 = mb + (fh >> 1) * 288 + (fh & 1) * 8;
    w64(w1 + fr * 16, u32x2{pk(p1[0], p1[1]), pk(p1[2], p1[3])});
    if (own) w64(w1 + 256 + (fr & 1) * 16, u32x2{pk(p2[0], p2[1]), pk(p2[2], p2[3])});
    if (fh == (hi >> 1)) {
      w32(mb + 576 + fr * 4, (hi & 1) ? pk(p3[2], p3[3]) : pk(p3[0], p3[1]));
      if (own) w32(mb + 640 + (fr & 1) * 4, (hi & 1) ? pk(p4[2], p4[3]) : pk(p4[0], p4[1]));
    }
    CFENCE();
    const u32x2 q0 = r64(mb + lane * 8), q1 = r64(mb + 512 + lane * 8);
    st_sc1(rs, d0, u32x4{q0[0], t0, q0[1], t0});
    st_sc1(rs, d1, u32x4{q1[0], t1, q1[1], t1});
  }
  STAMP(5);
  kv_consume(L, rs, rb, g, tme, 4 + hi, lane STPASS);
}

DI void role_q(lp L, char* ws, int jq, int hg, int rg, int lane STARG) {
  const int fh = lane >> 4;
  const rsrc_t rs = wsrsrc(ws);
  f32x4 acc[3][9];
  gemm_main<1>(L, rg, lane, acc STPASS);
  STAMP(3);
#pragma unroll
  for (int i = 0; i < 3; ++i)
#pragma unroll
    for (int t = 0; t < 9; ++t) { acc[i][t] = phi4(acc[i][t]); PIN(acc[i][t]); }
  const int rt0 = jq * 12 + rg * 3;
  u32 po = WS_PQ + rt0 * (18 * 1024) + hg * 4096 + lane * 16;
  u32 pe = WS_PQ + (rt0 + (lane >> 5)) * (18 * 1024) + 16384 + hg * 512 + (lane & 31) * 16;
  PIN(po); PIN(pe);
  STAMP(7);
  BAR();
  STAMP(8);
  f32x4 z[9];
#pragma unroll
  for (int t = 0; t < 9; ++t) z[t] = __builtin_bit_cast(f32x4, r128(L + GWL + fh * 160 + t * 16));
  u32x2 ex[3];
#pragma unroll
  for (int i = 0; i < 3; ++i) {
    const f32x4 x4 = acc[i][4] * z[8];
    f32x4 d4 = acc[i][0] * z[0] + acc[i][1] * z[1] + acc[i][2] * z[2] + acc[i][3] * z[3];
    f32x4 e4 = acc[i][5] * z[4] + acc[i][6] * z[5] + acc[i][7] * z[6] + acc[i][8] * z[7];
    f32x2 d2 = f32x2{d4[0], d4[1]} + f32x2{d4[2], d4[3]} + f32x2{x4[0], x4[1]};
    f32x2 e2 = f32x2{e4[0], e4[1]} + f32x2{e4[2], e4[3]} + f32x2{x4[2], x4[3]};
    const float r = __builtin_amdgcn_rcpf(d2[0] + d2[1] + EPS), s = __builtin_amdgcn_rcpf(e2[0] + e2[1] + EPS);
    const u32 p = rt0 + i < 128 ? po + i * (18 * 1024) : OOR;
    st_sc1(rs, p, pack2(acc[i][0] * r, acc[i][1] * r));
    st_sc1(rs, p + 1024, pack2(acc[i][2] * r, acc[i][3] * r));
    st_sc1(rs, p + 2048, pack2(acc[i][5] * s, acc[i][6] * s));
    st_sc1(rs, p + 3072, pack2(acc[i][7] * s, acc[i][8] * s));
    ex[i] = u32x2{pk(acc[i][4][0] * r, acc[i][4][1] * r), pk(acc[i][4][2] * s, acc[i][4][3] * s)};
    SCHED();
  }
#pragma unroll
  for (int k = 0; k < 2; ++k) {
    const int ib = k ? 2 : 1;
    auto sx = __builtin_amdgcn_permlane32_swap(ex[2 * k][0], ex[ib][0], false, false);
    auto sy = __builtin_amdgcn_permlane32_swap(ex[2 * k][1], ex[ib][1], false, false);
    const bool ok = rt0 + 2 * k + (lane >> 5) < 128 && (k == 0 || lane < 32);
    st_sc1(rs, ok ? pe + k * (36 * 1024) : OOR, u32x4{sx[0], sy[0], sx[1], sy[1]});
  }
}

struct cx2 { f32x2 r, i; };
DI cx2 cmul(cx2 a, cx2 b) { return cx2{a.r * b.r - a.i * b.i, a.r * b.i + a.i * b.r}; }
DI cx2 csq(cx2 a) { return cx2{a.r * a.r - a.i * a.i, (a.r + a.r) * a.i}; }
DI cx2 cpow(f32x2 lr, f32x2 rv, float n) {
  cx2 o;
#pragma unroll
  for (int m = 0; m < 2; ++m) {
    float mag = __builtin_amdgcn_exp2f(lr[m] * n), ph = __builtin_amdgcn_fractf(rv[m] * n);
    o.r[m] = mag * __builtin_amdgcn_cosf(ph);
    o.i[m] = mag * __builtin_amdgcn_sinf(ph);
  }
  return o;
}
constexpr int UB = 4608, PIT = 544, STG = UB, TBL = STG + 16 * PIT, WVB = TBL + 4368;
constexpr int TPE = TBL, TPD = 1216, TPO = TBL + TPD, F2 = TBL + 2368, F2S = 144, F3 = F2 + 4 * F2S, F3S = 176, FCP = 960;
static_assert(8 * WVB <= K1_LDS - 256 && WVB % 16 == 0, "lds");
struct SsmTab { u32 v[9][64][4]; };
constexpr SsmTab make_tab() {
  SsmTab t{};
  for (int l = 0; l < 64; ++l) {
    const int fr = l & 15, fh = l >> 4;
    const int lo[5] = {3, 6, 5, 4, 3}, hi[5] = {5, 10, 11, 12, 4};
    for (int k = 0; k < 5; ++k)
      for (int jj = 0; jj < 8; ++jj) {
        const int xx = fr + 16 - 8 * fh - jj;
        if (xx >= lo[k] && xx <= hi[k]) t.v[k][l][jj >> 1] |= (k == 4 ? 0xffffu : 0x3f80u) << (16 * (jj & 1));
      }
    for (int j = 0; j < 4; ++j)
      t.v[5][l][j] = fh < 2 ? F2 + (2 * (fr & 1) + (j & 1)) * F2S + (47 - 16 * fh - 8 * (j >> 1) - (fr >> 1)) * 2
                            : F3 + (fr & 1) * F3S + (63 - 32 * (fh - 2) - 8 * j - (fr >> 1)) * 2;
    t.v[6][l][0] = TBL + 1080 - 2 * (128 * (fh >> 1) + 4 * (fh & 1) + 8 * fr);
    t.v[6][l][1] = TBL + 558 - 16 * (l & 31);
    t.v[6][l][2] = TBL + (l == 31 ? 60 : l == 63 ? 574 : 1088);
    t.v[6][l][3] = l < 32 ? l * 16 : l < 36 ? TPE + (l - 32) * 16 : l < 40 ? TPE + 1088 + (l - 36) * 16
                   : l < 44 ? TPO + (l - 40) * 16 : l < 48 ? TPO + 1088 + (l - 44) * 16 : F2;
    t.v[7][l][0] = fr * 32 + fh * 16;
    t.v[7][l][1] = ((fr & 1) ? TPO + 2 : TPE) + (16 - fr + 8 * fh) * 2;
    t.v[7][l][2] = F2 + ((fr & 1) ? 0 : FCP + 2) + (15 - fr + 4 * fh) * 2;
    t.v[7][l][3] = STG + fr * PIT + fh * 16;
    t.v[8][l][0] = (fh & 1) * 512 + (fr >> 2) * 128 + (fh >> 1) * 64 + (fr & 3) * 16;
  }
  return t;
}
__device__ const SsmTab ssm_tab = make_tab();

DI void role_ssm(lp L, const char* x, const u32* logA, const u32* Aim, const u32* Cre, const u32* Cim, const u16* logdt,
                 const u16* Dsk, char* ws, int cb, int wave, int lane STARG) {
  const int chn = cb * 8 + wave;
  const u32x4* tab = (const u32x4*)ssm_tab.v + lane;
  u32 pa, pi, pcr, pci, pdt, pds;
  u32x4 o5, o6, o7;
  asm volatile("global_load_dword %0, %9, %12\n\tglobal_load_dword %1, %9, %13\n\tglobal_load_ushort %4, %10, %16\n\t"
               "global_load_dword %2, %9, %14\n\tglobal_load_dword %3, %9, %15\n\tglobal_load_ushort %5, %10, %17\n\t"
               "global_load_dwordx4 %6, %11, %18\n\tglobal_load_dwordx4 %7, %11, %18 offset:1024\n\tglobal_load_dwordx4 %8, %11, %18 offset:2048"
               : "=&v"(pa), "=&v"(pi), "=&v"(pcr), "=&v"(pci), "=&v"(pdt), "=&v"(pds), "=&v"(o5), "=&v"(o6), "=&v"(o7)
               : "v"((chn * 64 + lane) * 4), "v"(chn * 2), "v"(lane * 16), "s"(logA), "s"(Aim), "s"(Cre), "s"(Cim), "s"(logdt), "s"(Dsk),
                 "s"(ssm_tab.v[5])
               : "memory");
  lp wv = L + wave * WVB;
  const int tid = wave * 64 + lane;
  w128(wv + F2 + lane * 16, u32x4{0, 0, 0, 0});
  if (lane < 56) w128(wv + F2 + 1024 + lane * 16, u32x4{0, 0, 0, 0});
  u32x4 ur[4];
  const char* xc = x + cb * 16;
  const u32 uo = 4 * tid * XROW;
  auto ldu = [&](int i) { SCHED(); ur[i] = *(const u32x4*)(xc + uo + i * XROW); SCHED(); };
  BAR();
  ldu(0);
  waitv<1>();
  asm volatile("" : "+v"(pa), "+v"(pi), "+v"(pcr), "+v"(pci), "+v"(pdt), "+v"(pds), "+v"(o5), "+v"(o6), "+v"(o7));
  const float ldt = bf2f(pdt), dsk = bf2f(pds);
  w128(wv + o6[3], u32x4{0, 0, 0, 0});
  lp fw[4] = {wv + o5[0], wv + o5[1], wv + o5[2], wv + o5[3]};
  lp e0 = wv + o6[0], e1 = wv + o6[1], e2 = wv + o6[2];
  lp ua = wv + o7[0], ta = wv + o7[1], t2 = wv + o7[2], fa = wv + o7[3], pw = wv + STG + lane * 8;
  CFENCE();
  STAMP(1);
  const float dt = rbf(__expf(ldt));
  f32x2 lr = {-rbf(__expf(bf2f(pa))) * dt, -rbf(__expf(bfhi(pa))) * dt};
  f32x2 rv = {bf2f(pi) * dt, bfhi(pi) * dt};
  lr *= L2E; rv *= 0.15915494309f;
  const cx2 w1 = cpow(lr, rv, 1.f), wb = cpow(lr, rv, 128.f);
  cx2 w8 = csq(csq(csq(w1)));
  w8.i = -w8.i;
  const cx2 cc = {f32x2{bf2f(pcr), bfhi(pcr)} * (2.f * dt), f32x2{bf2f(pci), bfhi(pci)} * (2.f * dt)};
  ldu(1);
  STAMP(2);
  auto put = [&](int row, cx2 v) { w64(pw + row * PIT, u32x2{pk(v.r[0], v.i[0]), pk(v.r[1], v.i[1])}); };
  auto get = [&](u32x4 (&f)[8]) {
    CFENCE();
#pragma unroll
    for (int s = 0; s < 8; ++s) f[s] = r128(fa + s * 64);
    CFENCE();
    SCHED();
  };
  const f32x4 zero = {0.f, 0.f, 0.f, 0.f};
  u32x4 yf[8], xf[8];
  {
    cx2 y = {f32x2{1.f, 1.f}, f32x2{0.f, 0.f}};
    put(0, y); y = w8; put(1, y);
#pragma unroll
    for (int r = 2; r < 16; ++r) { y = cmul(y, w8); put(r, y); if (r == 7) ldu(2); }
    get(yf);
    ldu(3);
  }
  STAMP(9);
  cx2 e[4];
  {
    cx2 v = cc, u8 = cmul(cc, wb);
#pragma unroll
    for (int a = 0; a < 8; ++a) {
      v = cmul(v, w1); u8 = cmul(u8, w1);
      put(a, v); put(8 + a, u8);
      if (a & 1) e[a >> 1] = u8;
    }
    get(xf);
  }
  STAMP(5);
  u32x4 xg[8], xh[8];
#pragma unroll
  for (int n = 0; n < 8; ++n) { e[n & 3] = cmul(e[n & 3], wb); put(n, e[n & 3]); }
  put(8, cc);
  get(xg);
#pragma unroll
  for (int b = 0; b < 4; ++b) {
    e[1] = cmul(e[1], wb); e[3] = cmul(e[3], wb);
    put(2 * b, e[1]); put(2 * b + 1, e[3]);
  }
#pragma unroll
  for (int n = 0; n < 8; ++n) { e[3] = cmul(e[3], wb); put(8 + n, e[3]); }
  get(xh);
  f32x4 k0 = zero, k1 = zero, k2 = zero;
#pragma unroll
  for (int n = 0; n < 8; ++n) { k0 = mfma(xf[n], yf[n], k0); k1 = mfma(xg[n], yf[n], k1); }
  SCHED();
#pragma unroll
  for (int n = 0; n < 8; ++n) k2 = mfma(xh[n], yf[n], k2);
  const u32 d32 = pk(k0[3], k0[2]);
  {
    const u32 d10 = pk(k0[1], k0[0]), d21 = pk(k0[2], k0[1]);
    w64(e0, u32x2{d32, d10});
    w16(e0 + TPD + 2, d32); w32(e0 + TPD + 4, d21); w16(e0 + TPD + 8, d10 >> 16);
  }
  {
    const u32 c33 = pk(k1[3], k1[3]), c32 = pk(k1[3], k1[2]), c11 = pk(k1[1], k1[1]), c10 = pk(k1[1], k1[0]), c21 = pk(k1[2], k1[1]);
    if (lane < 32) {
      w128(e1 + TPD + 2, u32x4{c33, c32, c11, c10});
      w16(e1, c33); w32(e1 + 2, c33); w32(e1 + 6, c21); w32(e1 + 10, c11); w16(e1 + 14, c10 >> 16);
    }
    if (lane == 31 || lane == 32 || lane == 63) {
      const u32 v = lane == 31 ? c33 : lane == 63 ? d32 : pk(k1[0] + dsk, 0.f);
      w16(e2, v); w16(e2 + TPD + 2, v);
    }
  }
  STAMP(3);
  u32x4 BX[5];
#pragma unroll
  for (int k = 0; k < 5; ++k) BX[k] = tab[k * 64];
  const u32 bo = ((const u32*)tab)[8 * 256];
  {
    const u32 f01 = pk(k2[0], k2[1]), f23 = pk(k2[2], k2[3]);
#pragma unroll
    for (int j = 0; j < 4; ++j) { const u32 v = (j < 2 ? f01 : f23) >> (16 * (j & 1)); w16(fw[j], v); w16(fw[j] + FCP + 2, v); }
  }
  const u32 k1024 = __builtin_amdgcn_readlane(pk(k2[3], k2[3]), 31);
  CFENCE();
  u32x4 T[10];
#pragma unroll
  for (int n = 0; n < 20; ++n) {
    const int k = n >> 1, hf = n & 1;
    const int o = k < 3 ? hf * F2S + 64 - 32 * k : k < 6 ? (2 + hf) * F2S + 64 - 32 * (k - 3) : 4 * F2S + hf * F3S + 96 - 32 * (k - 6);
    T[k][2 * hf] = r32(t2 + o); T[k][2 * hf + 1] = r32(t2 + o + 4);
  }
  const u32x4 F32 = r4x32(ta);
  STAMP(4);
#pragma unroll
  for (int c = 0; c < 4; ++c) {
    w64(L + (2 * c) * WVB + 512 + tid * 8, u32x2{__builtin_amdgcn_perm(ur[1][c], ur[0][c], 0x05040100), __builtin_amdgcn_perm(ur[3][c], ur[2][c], 0x05040100)});
    w64(L + (2 * c + 1) * WVB + 512 + tid * 8, u32x2{__builtin_amdgcn_perm(ur[1][c], ur[0][c], 0x07060302), __builtin_amdgcn_perm(ur[3][c], ur[2][c], 0x07060302)});
  }
  WAITL0();
  BAR();
  STAMP(6);
  u32x4 U[2][8], Fa[2], Fb[2];
  auto rdn = [&](int d0, int n) {
    const int q = d0 & 1;
    if (n == 0) { Fa[q][0] = r32(ta + 1024 - d0 * 64); Fa[q][1] = r32(ta + 1028 - d0 * 64); }
    else if (n == 1) { Fa[q][2] = r32(ta + 1032 - d0 * 64); Fa[q][3] = r32(ta + 1036 - d0 * 64); }
    else if (n < 10) U[q][n - 2] = r128(ua + 480 + (n - 2) * 512 - d0 * 64);
    else if (n == 10) { Fb[q][0] = r32(ta + 512 - d0 * 64); Fb[q][1] = r32(ta + 516 - d0 * 64); }
    else { Fb[q][2] = r32(ta + 520 - d0 * 64); Fb[q][3] = r32(ta + 524 - d0 * 64); }
  };
#pragma unroll
  for (int n = 0; n < 12; ++n) rdn(7, n);
  SCHED();
  u32x4 W1[6];
  f32x4 acc[8];
#pragma unroll
  for (int d0 = 7; d0 >= 0; --d0) {
    const int q = d0 & 1;
#pragma unroll
    for (int m = 0; m < 15; ++m) {
      if (d0 < 7 && m == 0) __builtin_amdgcn_s_waitcnt(0xC67F);
      if (d0 < 7 && m == 4) __builtin_amdgcn_s_waitcnt(0xC47F);
      if (m >= 8) acc[m - 7] = mfma(Fb[q], U[q][m - 8], acc[m - 7]);
      else acc[m] = mfma(Fa[q], U[q][m], d0 == 7 ? zero : acc[m]);
      SCHED();
      if (d0 > 0) { if (m < 12) rdn(d0 - 1, m); }
      else if (m < 6) W1[m] = r128(ua + 464 + m * 512);
      SCHED();
    }
  }
  STAMP(7);
  {
    const u32x4 FX = BX[4] & k1024;
    f32x4 s[6];
    u32x4 SA, SB, SC;
    auto box = [&](int b) {
      s[0] = mfma(U[0][b], BX[0], zero); s[1] = mfma(U[0][b], BX[1], zero);
      s[2] = mfma(W1[b], BX[0], zero); s[3] = mfma(W1[b], BX[1], zero);
      if (b < 4) { s[4] = mfma(U[0][b], BX[2], zero); s[5] = mfma(W1[b], BX[3], zero); }
    };
    box(0);
#pragma unroll
    for (int b = 0; b < 6; ++b) {
      acc[b + 2] = mfma(F32, U[0][b], acc[b + 2]);
      if (b < 4) acc[b + 4] = mfma(FX, U[0][b], acc[b + 4]);
      SA = pack2(s[0], s[1]); SB = pack2(s[2], s[3]);
      if (b < 4) SC = pack2(s[4], s[5]);
      if (b < 5) box(b + 1);
#pragma unroll
      for (int d = 0; d < 3; ++d)
        if (b + 2 + d < 8) acc[b + 2 + d] = mfma(SA, T[d], acc[b + 2 + d]);
#pragma unroll
      for (int d = 0; d < 3; ++d)
        if (b + 2 + d < 8) acc[b + 2 + d] = mfma(SB, T[3 + d], acc[b + 2 + d]);
      if (b < 4) {
#pragma unroll
        for (int d = 0; d < 4; ++d)
          if (b + 4 + d < 8) acc[b + 4 + d] = mfma(SC, T[6 + d], acc[b + 4 + d]);
      }
    }
#pragma unroll
    for (int i = 0; i < 76; ++i) { __builtin_amdgcn_sched_group_barrier(0x8, 1, 0); __builtin_amdgcn_sched_group_barrier(0x2, 1, 0); }
  }
  STAMP(8);
  const rsrc_t rs = wsrsrc(ws);
  const u32 dst = WS_BT + chn * 4096 + bo;
#pragma unroll
  for (int i = 0; i < 8; i += 2) {
    auto sx = __builtin_amdgcn_permlane16_swap(pk(acc[i][0], acc[i][1]), pk(acc[i + 1][0], acc[i + 1][1]), false, false);
    auto sy = __builtin_amdgcn_permlane16_swap(pk(acc[i][2], acc[i][3]), pk(acc[i + 1][2], acc[i + 1][3]), false, false);
    st_sc1(rs, dst + i * 512, u32x4{sx[0], sy[0], sx[1], sy[1]});
  }
}

DI void touch(const void* sbase, u32 voff) {
  asm volatile("s_mov_b32 m0, %2\n\ts_nop 0\n\tglobal_load_lds_dword %0, %1" ::"v"(voff), "s"(sbase), "s"(K1_LDS - 256) : "memory");
}
extern "C" __global__ __launch_bounds__(512, 2) void gm_k1(const char* x, const char* w, const char* wo, const u32* logA, const u32* Aim,
                                             const u32* Cre, const u32* Cim, const u16* logdt, const u16* Dsk, const char* gw, char* ws, const char* out) {
  extern __shared__ __attribute__((aligned(16))) char smem[];
  lp L = (lp)smem;
  STDECL;
  STAMP(0);
  const int xcd = blockIdx.x & 7, loc = (int)(blockIdx.x >> 3) - 1;
  const int wave = __builtin_amdgcn_readfirstlane(threadIdx.x >> 6), lane = threadIdx.x & 63;
  if (__builtin_expect(loc < 0, 0)) {
    const char* pc; u32 back, len;
    asm volatile("s_getpc_b64 %0\n1:\n\ts_mov_b32 %1, 1b-gm_k1\n\ts_mov_b32 %2, gm_code_end-gm_k1" : "=s"(pc), "=s"(back), "=s"(len));
    pc -= back;
    touch(pc, min((u32)threadIdx.x * 128, len));
    if (wave == 6) { touch(ws + WS_EP, lane * 128); touch(ws + WS_EP, min(8192 + lane * 128, 176 * 64 - 4)); }
    if (wave == 7) { touch(ws, min(lane * 131072, WS_END - 4)); touch(out, min(lane * 65536, SEQ * XROW - 4)); }
    const char* wsrc = wo + (xcd & 1) * (16 * 18 * XROW);
#pragma unroll
    for (int i = 0; i < 5; ++i) touch(wsrc + i * 65536, threadIdx.x * 128);
    u32x4 gv[9];
#pragma unroll
    for (int i = 0; i < 9; ++i) gv[i] = *(const u32x4*)(gw + i * 128 + (lane & 7) * 16);
    float res = 0.f;
#pragma unroll
    for (int p = 0; p < 4; ++p) {
      const char* xp = x + (u32)(xcd * 256 + wave * 32 + (lane >> 3) * 4 + p) * XROW + (lane & 7) * 16;
      u32x4 xv[9];
#pragma unroll
      for (int i = 0; i < 9; ++i) xv[i] = *(const u32x4*)(xp + i * 128);
      float a = 0.f;
#pragma unroll
      for (int i = 0; i < 9; ++i)
#pragma unroll
        for (int d = 0; d < 4; ++d) {
          const u32 xa = xv[i][d], ga = gv[i][d];
          a = __builtin_amdgcn_fdot2_f32_bf16(__builtin_bit_cast(bf16x2, xa), __builtin_bit_cast(bf16x2, ga), a, false);
        }
      a += __builtin_bit_cast(float, __builtin_amdgcn_update_dpp(0, __builtin_bit_cast(int, a), 0xb1, 0xf, 0xf, false));
      a += __builtin_bit_cast(float, __builtin_amdgcn_update_dpp(0, __builtin_bit_cast(int, a), 0x4e, 0xf, 0xf, false));
      a += __builtin_bit_cast(float, __builtin_amdgcn_update_dpp(0, __builtin_bit_cast(int, a), 0x141, 0xf, 0xf, false));
      res = (lane & 3) == p ? a : res;
    }
    res = rbf(__builtin_amdgcn_rcpf(1.f + __builtin_amdgcn_exp2f(-L2E * rbf(res))));
    res = __int_as_float(__builtin_amdgcn_ds_bpermute(((lane >> 2) * 8 + (lane & 3)) * 4, __float_as_int(res)));
    st32_sc1(wsrsrc(ws), lane < 32 ? WS_G + (xcd * 256 + wave * 32 + lane) * 4 : OOR, __float_as_uint(res));
    return;
  }
  const bool isq = loc >= 25;
  const int Q = xcd >> 1, l = loc - 25;
  const int rq = ((0x8530 >> (4 * Q)) & 15) + (l >> 1), rk = Q * 4 + (loc >> 2);
  const int cq = (xcd & 1) * 2 + (l & 1), ck = (xcd & 1) * 4 + (loc & 3);
  const int sq = 3 * (l >> 1) + 4 * (l & 1) + 1, sk = 2 * ((loc >> 2) + (loc & 3));
  int rbi = isq ? rq : rk, cg = isq ? cq : ck, c0 = isq ? sq : sk;
  c0 = c0 >= 9 ? c0 - 9 : c0;
  asm volatile("" : "+s"(rbi), "+s"(cg), "+s"(c0));
  PINS(x); PINS(w); PINS(wo); PINS(logA); PINS(Aim); PINS(Cre); PINS(Cim); PINS(logdt); PINS(Dsk); PINS(gw); PINS(ws);
  if (Q == 1 && l >= 4) return;
  if (__builtin_expect((loc < 16 || isq) && wave < 4, 1)) {
    role_load(L, x, w, wo, ws, isq, rbi, cg, c0, wave, lane STPASS);
  } else if (loc < 16) {
    role_kv(L, ws, rbi, cg, wave - 4, lane STPASS);
  } else if (isq) {
    role_q(L, ws, rbi, cg, wave - 4, lane STPASS);
  } else {
    role_ssm(L, x, logA, Aim, Cre, Cim, logdt, Dsk, ws, xcd * 9 + loc - 16, wave, lane STPASS);
  }
  STFLUSH(0);
}

constexpr int K2S = 27648, K2B = 5 * K2S, K2G = K2B + 10240, SP = 2;
template <int NR>
DI void k2_compute(lp L, char* out, int rt0, int r0, int cb, int ch, int lane STARG) {
  const int fr = lane & 15, fh = lane >> 4;
  const int col = 32 * ch + 2 * fr;
  lp B0 = L + fh * 1024 + ((col ^ (col >> 4)) << 4), B1 = L + fh * 1024 + (((col + 1) ^ (col >> 4)) << 4);
  lp A = L + 12288 + r0 * 3072 + lane * 16;
  lp Be = L + SP * K2S + fh * 256 + col * 4, Ae = L + SP * K2S + 12288 + r0 * 3072 + (lane & 31) * 16 + (lane >> 5) * 8;
  PIN(B0); PIN(B1); PIN(A); PIN(Be); PIN(Ae); PINS(out);
  f32x4 acc[NR][2], cst[NR][2], g[NR];
#pragma unroll
  for (int sl = 0; sl < 6; ++sl) {
    const int so = (sl == 5 ? 0 : sl) * K2S;
    lp b0 = B0, b1 = B1, a0 = A;
    if (so > 32768) { b0 += 2 * K2S; b1 += 2 * K2S; a0 += 2 * K2S; }
    const int o = so > 32768 ? so - 2 * K2S : so;
    BAR();
    STAMP(1 + sl);
    u32x4 a[3][NR], b[3][2];
#pragma unroll
    for (int s = 0; s < 3; ++s) {
      if (sl == SP && s > 0) {
#pragma unroll
        for (int t = 0; t < 2; ++t) { lp e = Be + s * 4096 + t * 4; b[s][t] = u32x4{r32(e), r32(e + 1024), r32(e + 2048), r32(e + 3072)}; }
#pragma unroll
        for (int i = 0; i < NR; ++i) {
          const u32x2 lo = r64(Ae + i * 3072 + s * 1024), hi = r64(Ae + i * 3072 + s * 1024 + 512);
          a[s][i] = u32x4{lo[0], lo[1], hi[0], hi[1]};
        }
      } else {
        b[s][0] = r128(b0 + o + s * 4096);
#pragma unroll
        for (int i = 0; i < NR; ++i) a[s][i] = r128(a0 + o + i * 3072 + s * 1024);
        SCHED();
        b[s][1] = r128(b1 + o + s * 4096);
      }
      SCHED();
    }
#pragma unroll
    for (int s = 0; s < 3; ++s)
#pragma unroll
      for (int t = 0; t < 2; ++t)
#pragma unroll
        for (int i = 0; i < NR; ++i) acc[i][t] = mfma(a[s][i], b[s][t], sl + s ? acc[i][t] : f32x4{0.f, 0.f, 0.f, 0.f});
    SCHED();
    if (sl == 3) {
#pragma unroll
      for (int i = 0; i < NR; ++i) {
        g[i] = __builtin_bit_cast(f32x4, r128(L + K2G + (r0 + i) * 64 + fh * 16));
#pragma unroll
        for (int t = 0; t < 2; ++t) {
          const u32x2 bv = r64(L + K2B + (col + t) * 160 + ((fh >> 1) * 5 + r0 + i) * 16 + (fh & 1) * 8);
          cst[i][t] = f32x4{bf2f(bv[0]), bfhi(bv[0]), bf2f(bv[1]), bfhi(bv[1])} * (1.f - g[i]);
          PIN(cst[i][t]);
        }
      }
    }
  }
  STAMP(7);
  const rsrc_t ro = __builtin_amdgcn_make_buffer_rsrc(out, 0, SEQ * XROW, 0x00020000);
  const u32 dst = ((rt0 + r0) * 16 + fh * 4) * XROW + cb * 128 + col * 2;
#pragma unroll
  for (int i = 0; i < NR; ++i) {
    const f32x4 u = acc[i][0] * g[i] + cst[i][0], v = acc[i][1] * g[i] + cst[i][1];
#pragma unroll
    for (int j = 0; j < 4; ++j) __builtin_amdgcn_raw_buffer_store_b32(pk(u[j], v[j]), ro, dst + (16 * i + j) * XROW, 0, AUX_OUT);
  }
}

extern "C" __global__ __launch_bounds__(512, 2) void gm_k2(const char* ws, char* out) {
  extern __shared__ __attribute__((aligned(16))) char smem[];
  lp L = (lp)smem;
  STDECL;
  STAMP(0);
  const int xcd = blockIdx.x & 7, id = xcd * 29 + (xcd < 2 ? xcd : 2) + (blockIdx.x >> 3);
  const int rb = (id * 7282) >> 16, cb = id - rb * 9;
  const int wave = __builtin_amdgcn_readfirstlane(threadIdx.x >> 6), lane = threadIdx.x & 63;
  if (__builtin_expect(wave < 4, 1)) {
    __builtin_amdgcn_s_setprio(2);
    const u32 ord = (u32)(0x104321043210ull >> (4 * ((cb + 2 * rb) & 7)));
    const u32 lo = lane * 16;
    const int t1 = min(5 * rb + wave, 127), t4 = min(5 * rb + 4, 127);
    const char* mb = ws + (WS_M + cb * 73728 + wave * 3072);
    const char* p1 = ws + (WS_PQ + t1 * 18432);
    const char* p4 = ws + (WS_PQ + t4 * 18432 + wave * 768);
    auto issue = [&](int sl) {
      const u32 gq = sl == SP ? 5 : (ord >> (4 * (sl - (sl > SP)))) & 15;
      const u32 lb = (sl == 5 ? 0 : sl) * K2S;
      glds3(mb + gq * 12288, lb + wave * 3072, lo);
      glds3(p1 + gq * 3072, lb + 12288 + wave * 3072, lo);
      asm volatile("s_mov_b32 m0, %2\n\ts_mov_b32 exec_hi, 0xffff\n\tglobal_load_lds_dwordx4 %0, %1\n\ts_mov_b32 exec_hi, -1"
                   ::"v"(lo), "s"(p4 + gq * 3072), "s"(lb + 24576 + wave * 768) : "memory");
    };
    issue(0); issue(1);
    waitv<7>(); BAR();
    issue(2);
    u32 bo[3];
#pragma unroll
    for (int i = 0; i < 3; ++i) {
      const int f = wave * 160 + i * 64 + (i < 2 ? lane : lane & 31);
      const int ir = (f * 6554) >> 16, pc = f - ir * 10, hh = pc >= 5;
      const int rt = min(5 * rb + pc - 5 * hh, 127);
      bo[i] = ir * 4096 + (rt >> 2) * 128 + hh * 64 + (rt & 3) * 16;
    }
    waitv<7>(); BAR();
    {
      const char* bt = ws + (WS_BT + cb * (64 * 4096));
      glds(bt, bo[0], K2B + wave * 2560); glds(bt, bo[1], K2B + wave * 2560 + 1024);
      glds_lo(bt, bo[2], K2B + wave * 2560 + 2048);
      if (wave == 0 && lane < 20) glds(ws + WS_G, min(rb * 320 + lane * 16, SEQ * 4 - 16), K2G);
    }
    issue(3); waitv<10>(); BAR();
    issue(4); waitv<7>(); BAR();
    issue(5); waitv<7>(); BAR();
    waitv<0>(); BAR();
  } else if (wave < 6) {
    k2_compute<3>(L, out, 5 * rb, 0, cb, wave & 1, lane STPASS);
  } else {
    k2_compute<2>(L, out, 5 * rb, 3, cb, wave & 1, lane STPASS);
  }
  STFLUSH(1);
}

extern "C" __device__ __attribute__((used, noinline)) void gm_code_end() {}

extern "C" void kernel_launch(void* const* d_in, const int* in_sizes, int n_in, void* d_out, int out_size, void* d_ws,
                              size_t ws_size, hipStream_t stream) {
  (void)hipFuncSetAttribute((const void*)gm_k1, hipFuncAttributeMaxDynamicSharedMemorySize, K1_LDS);
  (void)hipFuncSetAttribute((const void*)gm_k2, hipFuncAttributeMaxDynamicSharedMemorySize, K2_LDS);
  gm_k1<<<256, 512, K1_LDS, stream>>>((const char*)d_in[0], (const char*)d_in[1], (const char*)d_in[2], (const u32*)d_in[4],
                                   (const u32*)d_in[5], (const u32*)d_in[6], (const u32*)d_in[7], (const u16*)d_in[8],
                                   (const u16*)d_in[9], (const char*)d_in[10], (char*)d_ws, (const char*)d_out);
  gm_k2<<<234, 512, K2_LDS, stream>>>((const char*)d_ws, (char*)d_out);
}
```

```cpp
#include <hip/hip_runtime.h>
#include <stdint.h>
#define PAD1 0
#define PAD2 1000

typedef unsigned int u32;
typedef unsigned short u16;
typedef unsigned long long u64;
typedef __attribute__((ext_vector_type(2))) unsigned u32x2;
typedef __attribute__((ext_vector_type(4))) unsigned u32x4;
typedef __attribute__((ext_vector_type(2))) float f32x2;
typedef __attribute__((ext_vector_type(4))) float f32x4;
typedef __attribute__((ext_vector_type(4))) short s16x4;
typedef __attribute__((ext_vector_type(2))) __bf16 bf16x2;
typedef __attribute__((ext_vector_type(8))) __bf16 bf16x8;
#define LAS __attribute__((address_space(3)))
#define GAS __attribute__((address_space(1)))
#define DI __device__ __forceinline__
typedef LAS char* lp;

constexpr int SEQ = 2048, DM = 576;
constexpr int XROW = DM * 2;
constexpr int WROW = 3 * DM * 2;
constexpr int WS_PQ = 0;
constexpr int WS_M = WS_PQ + 128 * 18 * 1024;
constexpr int WS_BT = WS_M + 36 * 18 * 1024;
constexpr int WS_G = WS_BT + DM * SEQ * 2;
constexpr int WS_MBKV = WS_G + SEQ * 4;
constexpr int MBKV_SZ = 16 * 36 * 16;
constexpr int WS_MBZ = WS_MBKV + 128 * MBKV_SZ;
constexpr int MBZ_SZ = 2 * 16 * 20 * 16;
constexpr int WS_EP = WS_MBZ + 44 * MBZ_SZ;
constexpr int WS_END = WS_EP + 176 * 64;
constexpr int K1_LDS = 162304;
constexpr int K2_LDS = 5 * 27648 + 10240 + 320;
#define PF_BLOCKS 1
#define AUX_OUT 16

DI float bf2f(u32 v) { return __uint_as_float(v << 16); }
DI float bfhi(u32 v) { return __uint_as_float(v & 0xffff0000u); }
DI u32 pk(float a, float b) { bf16x2 v = {(__bf16)a, (__bf16)b}; return __builtin_bit_cast(u32, v); }
DI float rbf(float a) { return bf2f(pk(a, a) & 0xffffu); }
DI f32x4 mfma(u32x4 a, u32x4 b, f32x4 c) {
  return __builtin_amdgcn_mfma_f32_16x16x32_bf16(__builtin_bit_cast(bf16x8, a), __builtin_bit_cast(bf16x8, b), c, 0, 0, 0);
}
DI u32x4 r128(lp a) { return *(LAS u32x4*)a; }
DI u32x2 r64(lp a) { return *(LAS u32x2*)a; }
DI u32 r32(lp a) { return *(LAS u32*)a; }
DI void w128(lp a, u32x4 v) { *(LAS u32x4*)a = v; }
DI void w64(lp a, u32x2 v) { *(LAS u32x2*)a = v; }
DI void w32(lp a, u32 v) { *(LAS u32*)a = v; }
DI void w16(lp a, u32 v) { *(LAS u16*)a = (u16)v; }
DI u32x2 rtr(lp a) { return __builtin_bit_cast(u32x2, __builtin_amdgcn_ds_read_tr16_b64_v4i16((LAS s16x4*)a)); }
DI u32x4 rtr2(lp a0, lp a1) { u32x2 l = rtr(a0), h = rtr(a1); return u32x4{l[0], l[1], h[0], h[1]}; }
DI u32x4 r4x32(lp a) { return u32x4{r32(a), r32(a + 4), r32(a + 8), r32(a + 12)}; }
DI u32x4 r2x2(lp a, lp b) { return u32x4{r32(a), r32(a + 4), r32(b), r32(b + 4)}; }
DI void glds(const void* sbase, u32 voff, u32 lds) {
  asm volatile("s_mov_b32 m0, %2\n\ts_nop 0\n\tglobal_load_lds_dwordx4 %0, %1" ::"v"(voff), "s"(sbase), "s"(lds) : "memory");
}
DI void glds4(const void* sbase, u32 lds, u32 v0, u32 v1, u32 v2, u32 v3) {
  asm volatile("s_mov_b32 m0, %5\n\ts_nop 0\n\tglobal_load_lds_dwordx4 %0, %4\n\tglobal_load_lds_dwordx4 %1, %4 offset:1024\n\t"
               "global_load_lds_dwordx4 %2, %4 offset:2048\n\tglobal_load_lds_dwordx4 %3, %4 offset:3072"
               ::"v"(v0), "v"(v1), "v"(v2), "v"(v3), "s"(sbase), "s"(lds) : "memory");
}
DI void glds2(const void* sbase, u32 lds, u32 v0, u32 v1) {
  asm volatile("s_mov_b32 m0, %3\n\ts_nop 0\n\tglobal_load_lds_dwordx4 %0, %2\n\tglobal_load_lds_dwordx4 %1, %2 offset:1024"
               ::"v"(v0), "v"(v1), "s"(sbase), "s"(lds) : "memory");
}
DI void glds3(const void* sbase, u32 lds, u32 v) {
  asm volatile("s_mov_b32 m0, %2\n\ts_nop 0\n\tglobal_load_lds_dwordx4 %0, %1\n\tglobal_load_lds_dwordx4 %0, %1 offset:1024\n\t"
               "global_load_lds_dwordx4 %0, %1 offset:2048" ::"v"(v), "s"(sbase), "s"(lds) : "memory");
}
DI void glds_lo(const void* sbase, u32 voff, u32 lds) {
  asm volatile("s_mov_b32 m0, %2\n\ts_mov_b32 exec_hi, 0\n\tglobal_load_lds_dwordx4 %0, %1\n\ts_mov_b32 exec_hi, -1"
               ::"v"(voff), "s"(sbase), "s"(lds) : "memory");
}
template <int N> DI void waitv() { asm volatile("s_waitcnt vmcnt(%0)" ::"i"(N) : "memory"); }
#define CFENCE() asm volatile("" ::: "memory")
#define WAITL0() do { __builtin_amdgcn_s_waitcnt(0xC07F); CFENCE(); } while (0)
#define BAR() do { CFENCE(); __builtin_amdgcn_s_barrier(); CFENCE(); } while (0)
#define SCHED() __builtin_amdgcn_sched_barrier(0)
#define PIN(x) asm volatile("" : "+v"(x))
#define PINS(x) asm volatile("" ::"s"(x))
constexpr float EPS = 1e-6f, L2E = 1.44269504089f;
DI f32x2 phi2(float a0, float a1) {
  f32x2 m = f32x2{a0, a1} * L2E - EPS * L2E;
  f32x2 e = {__builtin_amdgcn_fmed3f(__builtin_amdgcn_exp2f(m[0]), 0.f, 1.f), __builtin_amdgcn_fmed3f(__builtin_amdgcn_exp2f(m[1]), 0.f, 1.f)};
  const int ei = __builtin_bit_cast(int, EPS);
  f32x2 x = {__int_as_float(max(__float_as_int(a0), ei)), __int_as_float(max(__float_as_int(a1), ei))};
  return x + e;
}
DI f32x4 phi4(f32x4 a) { f32x2 l = phi2(a[0], a[1]), h = phi2(a[2], a[3]); return f32x4{l[0], l[1], h[0], h[1]}; }
DI float rowsum16(float v) {
  v += __builtin_bit_cast(float, __builtin_amdgcn_update_dpp(0, __builtin_bit_cast(int, v), 0x128, 0xf, 0xf, false));
  v += __builtin_bit_cast(float, __builtin_amdgcn_update_dpp(0, __builtin_bit_cast(int, v), 0x124, 0xf, 0xf, false));
  v += __builtin_bit_cast(float, __builtin_amdgcn_update_dpp(0, __builtin_bit_cast(int, v), 0x122, 0xf, 0xf, false));
  v += __builtin_bit_cast(float, __builtin_amdgcn_update_dpp(0, __builtin_bit_cast(int, v), 0x121, 0xf, 0xf, false));
  return v;
}
DI float prodsum(float a, float b, float c, float d) {
  auto s0 = __builtin_amdgcn_permlane32_swap(__float_as_uint(a), __float_as_uint(c), false, false);
  auto s1 = __builtin_amdgcn_permlane32_swap(__float_as_uint(b), __float_as_uint(d), false, false);
  const float ac = __uint_as_float(s0[0]) + __uint_as_float(s0[1]), bd = __uint_as_float(s1[0]) + __uint_as_float(s1[1]);
  auto s2 = __builtin_amdgcn_permlane16_swap(__float_as_uint(ac), __float_as_uint(bd), false, false);
  float v = __uint_as_float(s2[0]) + __uint_as_float(s2[1]);
  v += __builtin_bit_cast(float, __builtin_amdgcn_update_dpp(0, __builtin_bit_cast(int, v), 0x128, 0xf, 0xf, false));
  v += __builtin_bit_cast(float, __builtin_amdgcn_update_dpp(0, __builtin_bit_cast(int, v), 0x124, 0xf, 0xf, false));
  return v;
}
DI float prodsum(u32x4 m) { return prodsum(bf2f(m[0]), bfhi(m[0]), bf2f(m[2]), bfhi(m[2])); }
DI u32x4 pack2(f32x4 a, f32x4 b) { return u32x4{pk(a[0], a[1]), pk(a[2], a[3]), pk(b[0], b[1]), pk(b[2], b[3])}; }
typedef __amdgpu_buffer_rsrc_t rsrc_t;
DI rsrc_t wsrsrc(char* ws) { return __builtin_amdgcn_make_buffer_rsrc(ws, 0, WS_END, 0x00020000); }
DI void st_sc1(rsrc_t r, u32 off, u32x4 v) { __builtin_amdgcn_raw_buffer_store_b128(v, r, off, 0, 16); }
DI u32x4 ld_sc1(rsrc_t r, u32 off) { return __builtin_amdgcn_raw_buffer_load_b128(r, off, 0, 16); }
DI void st32_sc1(rsrc_t r, u32 off, u32 v) { __builtin_amdgcn_raw_buffer_store_b32(v, r, off, 0, 16); }
DI u32 ld_ep(char* ws, int i) { return __hip_atomic_load((GAS u32*)(ws + WS_EP) + 16 * i, __ATOMIC_RELAXED, __HIP_MEMORY_SCOPE_AGENT); }
DI void st_ep(rsrc_t r, int i, int lane, u32 v) { if (lane < 4) st_sc1(r, WS_EP + 64 * i + 16 * lane, u32x4{v, v, v, v}); }
constexpr u32 OOR = 0x7ffffff0u;
constexpr u32 SPIN_CAP = 1u << 15;
#define POLL_SLEEP 4
#define STDECL
#define STARG
#define STPASS
#define STAMP(i)
#define STFLUSH(k)

constexpr int XR_KV = 4 * 18432, XR_Q = 3 * 18432;
constexpr int GWL = XR_KV + 4 * 16384;
constexpr int WOI = GWL + 1280;
constexpr int XCH = XR_KV + 16384, MSG = 18432, KVA = MSG + 4096;

template <int ROLE>
DI void gemm_main(lp L, int rg, int lane, f32x4 (&acc)[ROLE ? 3 : 2][9] STARG) {
  constexpr int RT = ROLE ? 3 : 2, XB = RT * 8192, XRING = ROLE ? XR_Q : XR_KV, NS = ROLE ? 3 : 4;
  const int fr = lane & 15, fh = lane >> 4;
  const int q4 = (lane >> 2) & 3, p4 = lane & 3;
  const int ax0 = XRING + (rg * RT * 16 + fr) * 128 + ((fh ^ ((fr >> 1) & 7)) << 4), ax1 = ax0 ^ 64;
  const int r0 = (8 * fh + q4 + 4 * (fh & 1)) * 288, r1 = (8 * fh + q4 + 4 * ((fh & 1) ^ 1)) * 288;
  const int cm = ROLE ? 72 * p4 : 8 * p4, cx = 72 * p4 + 32;
  u32x4 a0[RT], a1[RT], b0[9], b1[9];
  lp W0 = L + r0 + cm, W1 = L + r1 + cm, E0 = L + r0 + cx, E1 = L + r1 + cx, X0 = L + ax0, X1 = L + ax1;
  PIN(E0); PIN(E1);
  auto step = [&](bool domma, u32x4 (&a)[RT], u32x4 (&b)[9], int slot, int s, u32x4 (&an)[RT], u32x4 (&bn)[9]) {
    auto rd = [&](int n) {
      if (n < RT) { an[n] = r128((s ? X1 : X0) + slot * XB + n * 2048); return; }
      n -= RT;
      if (n >= 18) return;
      const int t = n >> 1, so = slot * 18432 + s * 9216;
      const int to = (ROLE ? 8 * t : 72 * (t >> 1) + 40 * (t & 1)) + so;
      const u32x2 v = (ROLE || t < 8) ? rtr((n & 1 ? W1 : W0) + to) : rtr((n & 1 ? E1 : E0) + so);
      bn[t][2 * (n & 1)] = v[0]; bn[t][2 * (n & 1) + 1] = v[1];
    };
    const int EX = ROLE ? 0 : (s ? 7 : 2);
#pragma unroll
    for (int m = 0; m < 9 * RT; ++m) {
      const int t = m / RT, i = m % RT;
      if (domma) acc[i][t] = ROLE ? mfma(b[t], a[i], acc[i][t]) : mfma(a[i], b[t], acc[i][t]);
      SCHED();
      if (m < 2 * EX) { rd(m + (m + 1) / 2); if (!(m & 1)) rd(m + m / 2 + 1); } else rd(m + EX);
      SCHED();
    }
  };
#pragma unroll
  for (int i = 0; i < RT; ++i)
#pragma unroll
    for (int t = 0; t < 9; ++t) { const float v = (ROLE || t < 4 || (t == 8 && fr < 8)) ? EPS : 0.f; acc[i][t] = f32x4{v, v, v, v}; PIN(acc[i][t]); }
  STAMP(1);
  BAR();
  STAMP(2);
  step(false, a1, b1, 0, 0, a0, b0);
#pragma unroll 1
  for (int it = 0; it < (ROLE ? 3 : 2); ++it) {
#pragma unroll
    for (int sl = 0; sl < NS; ++sl) {
      step(true, a0, b0, sl, 1, a1, b1);
      WAITL0();
      BAR();
      SCHED();
      step(true, a1, b1, sl == NS - 1 ? 0 : sl + 1, 0, a0, b0);
    }
  }
  if (ROLE == 0) {
    step(true, a0, b0, 0, 1, a1, b1);
#pragma unroll
    for (int t = 0; t < 9; ++t)
#pragma unroll
      for (int i = 0; i < RT; ++i) acc[i][(t + 8) % 9] = mfma(a1[i], b1[(t + 8) % 9], acc[i][(t + 8) % 9]);
  }
}

DI void kv_consume(lp L, rsrc_t rs, int rb, int g, u32 tme, int wave, int lane STARG) {
  const int chead = 4 * g + (rb & 3), crole = rb >> 2;
  if (crole == 3) return;
  const int fr = lane & 15, fh = lane >> 4;
  const bool ext = crole == 2;
  u32x4 wf[6];
  {
    const int codd = chead & 1, q4 = (lane >> 2) & 3, p4 = lane & 3;
    int s0 = 8 * fh + q4, s1 = s0 + 4;
    s0 = s0 > 17 ? 17 : s0; s1 = s1 > 17 ? 17 : s1;
    s0 += 2 * codd; s1 += 2 * codd;
    s0 = s0 >= 18 ? s0 - 18 : s0; s1 = s1 >= 18 ? s1 - 18 : s1;
    lp p0 = L + WOI + s0 * XROW + wave * 64 + p4 * 8, p1 = L + WOI + s1 * XROW + wave * 64 + p4 * 8;
#pragma unroll
    for (int i = 0; i < 6; ++i) wf[i] = rtr2(p0 + (i >> 1) * 512 + (i & 1) * 32, p1 + (i >> 1) * 512 + (i & 1) * 32);
  }
  const int un = 2 * (chead >> 3) + (chead & 1), hh = (chead >> 1) & 3;
  const int col = (wave & 1) * 32 + (lane & 31);
  u32 mo = WS_M + (wave >> 1) * 73728 + (ext ? 65536 + (un * 4 + hh) * 256 + col * 4 : (2 * un + crole) * 4096 + hh * 1024 + ((col ^ (col >> 4)) << 4));
  if (lane >= 32) mo = OOR;
  PIN(mo);
  STAMP(6);
  if (wave < (ext ? 3 : 8)) {
    const int it = 4 * wave + (lane & 3);
    const u32 off = WS_MBKV + (g * 16 + rb) * MBKV_SZ + (lane >> 2) * 576 + it * 16;
    const bool two = wave == 0 && !ext;
    lp d = L + KVA + (ext ? (fh & 1) * 64 + it * 4 + (fh >> 1) * 2 : ((it & 1) * 4 + fh) * 64 + (it >> 1) * 2);
    PIN(d);
    u32x4 m, e = {0, tme, 0, tme};
    for (u32 spin = 0;; ++spin) {
      m = ld_sc1(rs, off);
      if (two) e = ld_sc1(rs, off + 512);
      bool ok = m[1] == tme && m[3] == tme && e[1] == tme && e[3] == tme;
      if (__all(ok) || spin > SPIN_CAP) break;
      __builtin_amdgcn_s_sleep(POLL_SLEEP);
    }
    STAMP(7);
    const float sm = prodsum(m);
    if (fr < 4 && !(ext && it > 8)) w16(d, pk(sm, sm));
    if (two) { const float se = prodsum(e); if (fr < 4) w16(d + 32, pk(se, se)); }
  }
  WAITL0();
  BAR();
  STAMP(8);
  const u32x4 af = r128(L + KVA + fr * 64 + fh * 16);
#pragma unroll
  for (int i = 0; i < 3; ++i) {
    if (i == 2 && wave >= 2) break;
    const f32x4 u = mfma(af, wf[2 * i], f32x4{0.f, 0.f, 0.f, 0.f}), v = mfma(af, wf[2 * i + 1], f32x4{0.f, 0.f, 0.f, 0.f});
    auto sx = __builtin_amdgcn_permlane16_swap(pk(u[0], u[1]), pk(v[0], v[1]), false, false);
    auto sy = __builtin_amdgcn_permlane16_swap(pk(u[2], u[3]), pk(v[2], v[3]), false, false);
    if (ext) st32_sc1(rs, mo + i * (4 * 73728), sx[0]);
    else st_sc1(rs, mo + i * (4 * 73728), u32x4{sx[0], sy[0], sx[1], sy[1]});
  }
  if (wave == 7) st_ep(rs, g * 16 + rb, lane, tme);
}

DI void role_load(lp L, const char* x, const char* w, const char* wo_g, char* ws, bool isq, int rbi, int cg, int c0,
                  int lw, int lane STARG) {
  __builtin_amdgcn_s_setprio(2);
  const bool six = isq && rbi < 10;
  const int XP = isq ? 6 : 4;
  const char* xr = x + rbi * (isq ? 192 * XROW : 128 * XROW);
  const u32 xl = (isq ? XR_Q : XR_KV) + lw * XP * 1024, XB = XP * 4096;
  u32 xo[4], wo[5];
  xo[0] = (8 * (six || !isq ? lw * XP : (lw * XP > 12 ? 12 : lw * XP)) + (lane >> 3)) * XROW + (((lane & 7) ^ (lane >> 4)) << 4);
  xo[1] = (xo[0] ^ 64) + 8192; xo[2] = xo[0] + 16384; xo[3] = xo[1] + 16384;
  auto issx = [&](int cidx, int slot) {
    const char* xb = xr + cidx * 128;
    glds4(xb, xl + slot * XB, xo[0], xo[1], xo[2], xo[3]);
    if (isq) glds2(xb + (six || lw < 2 ? 32 * XROW : 0), xl + slot * XB + 4096, xo[0], xo[1]);
  };
  auto issw = [&](int cidx, int slot) {
    const char* wb = w + cidx * (64 * WROW);
    const u32 lb = slot * 18432 + lw * 4608;
    glds4(wb, lb, wo[0], wo[1], wo[2], wo[3]);
    glds_lo(wb, wo[4], lb + 4096);
  };
  auto nxt = [](int c) { return c == 8 ? 0 : c + 1; };
  STAMP(3);
  issx(c0, 0);
  SCHED();
  {
    const int cb0 = (isq ? 288 * cg : 1152 + 144 * cg) + 16 * lw * WROW, cb1 = isq ? cb0 : cb0 + 1008;
#pragma unroll
    for (int i = 0; i < 5; ++i) {
      const int f = i < 4 ? 64 * i + lane : 256 + (lane & 31);
      const int lr = (f * 3641) >> 16, cc = f - lr * 18;
      wo[i] = (lr ^ ((lr >> 1) & 4)) * WROW + (cc < 9 ? cb0 : cb1) + 16 * cc - (i < 4 ? 1024 * i : 0);
    }
  }
  issw(c0, 0);
  STAMP(9);
  int c = nxt(c0);
  const int fr = lane & 15, fh = lane >> 4;
  const int me = isq ? 128 + cg * 11 + rbi : cg * 16 + rbi;
  const int jq = lane >> 2, cz = (cg >> 1) * 11 + (jq > 10 ? 10 : jq);
  u32 tme, tz;
  if (isq) {
    issx(c, 1); issw(c, 1);
    c = nxt(c);
    int sx = 2;
    STAMP(1);
    waitv<11>();
    BAR();
    STAMP(2);
#pragma unroll 1
    for (int k = 0; k < 7; ++k) {
      issx(c, sx); issw(c, sx);
      c = nxt(c);
      sx = sx == 2 ? 0 : sx + 1;
      waitv<11>();
      BAR();
    }
    waitv<0>();
    tme = ld_ep(ws, me); tz = 0;
    BAR();
  } else {
    issx(c, 1); issw(c, 1);
    c = nxt(c);
    const char* wb = w + c * (64 * WROW);
    const char* xb = xr + c * 128;
    asm volatile("" : "+s"(wb), "+s"(xb));
    STAMP(1);
    waitv<9>();
    BAR();
    STAMP(2);
    glds4(xb, xl + 2 * 16384, xo[0], xo[1], xo[2], xo[3]);
#pragma unroll
    for (int k = 0; k < 7; ++k) {
      const u32 lb = ((k + 2) & 3) * 18432 + lw * 4608;
      glds4(wb, lb, wo[0], wo[1], wo[2], wo[3]);
      glds_lo(wb, wo[4], lb + 4096);
      if (k < 6) {
        c = nxt(c);
        xb = xr + c * 128; wb = w + c * (64 * WROW);
        asm volatile("" : "+s"(wb), "+s"(xb));
        glds4(xb, xl + ((k + 3) & 3) * 16384, xo[0], xo[1], xo[2], xo[3]);
        waitv<13>();
      } else {
        waitv<9>();
      }
      BAR();
    }
    if (rbi < 12) {
      const char* src = wo_g + (4 * cg + (rbi & 3)) * (18 * XROW) + lw * 5184;
      glds4(src, WOI + lw * 5184, lane * 16, lane * 16, lane * 16, lane * 16);
      glds(src + 4096, lane * 16, WOI + lw * 5184 + 4096);
      if (lane < 4) glds(src + 5120, lane * 16, WOI + lw * 5184 + 5120);
      waitv<6>();
    } else {
      waitv<0>();
    }
    BAR();
    tme = ld_ep(ws, me); tz = ld_ep(ws, 128 + cz);
  }
  const rsrc_t rs = wsrsrc(ws);
  tme += 1; tz += 1;
  if (isq) {
    BAR();
    const int i0 = lw * 4 + (lane & 3);
    const u32 base = WS_MBZ + (me - 128) * MBZ_SZ + (lane >> 2) * 320;
    u32 o[3]; lp zw[3];
#pragma unroll
    for (int i = 0; i < 3; ++i) {
      const int it = i0 + 16 * i, gi = it >= 20, r = it - 20 * gi;
      o[i] = base + gi * (MBZ_SZ / 2) + r * 16;
      zw[i] = L + GWL + (r < 16 ? (2 * gi + (r >> 3)) * 160 + (r & 7) * 16 : (2 * gi + (r & 1)) * 160 + 128 + (r & 2) * 8) + fh * 4;
    }
    u32x4 m[3];
    m[2] = u32x4{0, tme, 0, tme};
    for (u32 spin = 0;; ++spin) {
      bool ok = true;
      m[0] = ld_sc1(rs, o[0]); m[1] = ld_sc1(rs, o[1]);
      if (lw < 2) m[2] = ld_sc1(rs, o[2]);
#pragma unroll
      for (int i = 0; i < 3; ++i) ok = ok && m[i][1] == tme && m[i][3] == tme;
      if (__all(ok) || spin > SPIN_CAP) break;
      __builtin_amdgcn_s_sleep(POLL_SLEEP);
    }
    STAMP(7);
#pragma unroll
    for (int i = 0; i < 3; ++i) {
      if (i == 2 && lw >= 2) break;
      const float sv = prodsum(m[i]);
      if (fr < 4) w32(zw[i], __float_as_uint(sv));
    }
    WAITL0();
    BAR();
    if (lw == 0) st_ep(rs, me, lane, tme);
    return;
  }
  __builtin_amdgcn_s_setprio(0);
  if (lw == 0) w128(L + KVA + lane * 16, u32x4{0, 0, 0, 0});
  u32 zo = jq < 11 ? WS_MBZ + cz * MBZ_SZ + (cg & 1) * (MBZ_SZ / 2) + rbi * 320 + lw * 64 + (lane & 3) * 16 : OOR;
  PIN(zo);
  waitv<0>();
  WAITL0();
  BAR();
  STAMP(4);
  {
    const u32x4 one = {0x3f803f80u, 0x3f803f80u, 0x3f803f80u, 0x3f803f80u};
#pragma unroll
    for (int k = 0; k < 2; ++k) {
      if (k && lw) break;
      lp bk = L + XCH + lane * 16 + (k ? 8192 : lw * 1024);
      u32x4 kf[4];
#pragma unroll
      for (int r = 0; r < 4; ++r) kf[r] = r128(bk + r * 9216);
      const f32x4 t = mfma(kf[1], one, mfma(kf[0], one, f32x4{0.f, 0.f, 0.f, 0.f})) + mfma(kf[3], one, mfma(kf[2], one, f32x4{0.f, 0.f, 0.f, 0.f}));
      const u32 p0 = __builtin_amdgcn_ds_bpermute((lane & 3) * 64, pk(t[0], t[1])), p1 = __builtin_amdgcn_ds_bpermute((lane & 3) * 64, pk(t[2], t[3]));
      st_sc1(rs, zo + k * 256, u32x4{p0, tz, p1, tz});
    }
  }
  STAMP(5);
  kv_consume(L, rs, rbi, cg, tme, lw, lane STPASS);
}

DI void role_kv(lp L, char* ws, int rb, int g, int hi, int lane STARG) {
  const int fr = lane & 15, fh = lane >> 4;
  const rsrc_t rs = wsrsrc(ws);
  const int c0 = g * 16 + hi + (lane < 36 ? 0 : 4), c1 = g * 16 + hi + (lane < 8 ? 4 : 8);
  u32 d0 = WS_MBKV + c0 * MBKV_SZ + rb * 576 + (lane < 36 ? lane : lane - 36) * 16;
  u32 d1 = lane < 20 ? WS_MBKV + c1 * MBKV_SZ + rb * 576 + (lane < 8 ? lane + 28 : lane - 8) * 16 : OOR;
  PIN(d0); PIN(d1);
  f32x4 acc[2][9];
  gemm_main<0>(L, hi, lane, acc STPASS);
  u32 t0 = ld_ep(ws, c0), t1 = ld_ep(ws, c1), tme = ld_ep(ws, g * 16 + rb);
  STAMP(3);
  {
    lp xw = L + XCH + hi * 9216 + lane * 16;
#pragma unroll
    for (int t = 0; t < 4; ++t) {
      w128(xw + t * 1024, pack2(phi4(acc[0][t]), phi4(acc[1][t])));
      w128(xw + 4096 + t * 1024, pack2(acc[0][4 + t], acc[1][4 + t]));
    }
    const u32x4 xp = pack2(phi4(acc[0][8]), phi4(acc[1][8])), xr = pack2(acc[0][8], acc[1][8]);
    w128(xw + 8192, fr < 8 ? xp : xr);
  }
  t0 += 1; t1 += 1; tme += 1;
  lp mb = L + MSG + hi * 1024;
  WAITL0();
  BAR();
  STAMP(4);
  {
    f32x4 p1 = {0.f, 0.f, 0.f, 0.f}, p2 = p1, p3 = p1, p4 = p1;
    lp base = L + XCH + lane * 16;
    u32x4 xf[4], vf[4], kf[4];
#pragma unroll
    for (int r = 0; r < 4; ++r) { kf[r] = r128(base + hi * 1024 + r * 9216); vf[r] = r128(base + 4096 + hi * 1024 + r * 9216); xf[r] = r128(base + r * 9216 + 8192); }
#pragma unroll
    for (int r = 0; r < 4; ++r) {
      p1 = mfma(kf[r], vf[r], p1); p2 = mfma(kf[r], xf[r], p2);
      p3 = mfma(xf[r], vf[r], p3); p4 = mfma(xf[r], xf[r], p4);
    }
    const bool own = (fr >> 1) == 4 + hi;
    lp w1 = mb + (fh >> 1) * 288 + (fh & 1) * 8;
    w64(w1 + fr * 16, u32x2{pk(p1[0], p1[1]), pk(p1[2], p1[3])});
    if (own) w64(w1 + 256 + (fr & 1) * 16, u32x2{pk(p2[0], p2[1]), pk(p2[2], p2[3])});
    if (fh == (hi >> 1)) {
      w32(mb + 576 + fr * 4, (hi & 1) ? pk(p3[2], p3[3]) : pk(p3[0], p3[1]));
      if (own) w32(mb + 640 + (fr & 1) * 4, (hi & 1) ? pk(p4[2], p4[3]) : pk(p4[0], p4[1]));
    }
    CFENCE();
    const u32x2 q0 = r64(mb + lane * 8), q1 = r64(mb + 512 + lane * 8);
    st_sc1(rs, d0, u32x4{q0[0], t0, q0[1], t0});
    st_sc1(rs, d1, u32x4{q1[0], t1, q1[1], t1});
  }
  STAMP(5);
  kv_consume(L, rs, rb, g, tme, 4 + hi, lane STPASS);
}

DI void role_q(lp L, char* ws, int jq, int hg, int rg, int lane STARG) {
  const int fh = lane >> 4;
  const rsrc_t rs = wsrsrc(ws);
  f32x4 acc[3][9];
  gemm_main<1>(L, rg, lane, acc STPASS);
  STAMP(3);
#pragma unroll
  for (int i = 0; i < 3; ++i)
#pragma unroll
    for (int t = 0; t < 9; ++t) { acc[i][t] = phi4(acc[i][t]); PIN(acc[i][t]); }
  const int rt0 = jq * 12 + rg * 3;
  u32 po = WS_PQ + rt0 * (18 * 1024) + hg * 4096 + lane * 16;
  u32 pe = WS_PQ + (rt0 + (lane >> 5)) * (18 * 1024) + 16384 + hg * 512 + (lane & 31) * 16;
  PIN(po); PIN(pe);
  STAMP(7);
  BAR();
  STAMP(8);
  f32x4 z[9];
#pragma unroll
  for (int t = 0; t < 9; ++t) z[t] = __builtin_bit_cast(f32x4, r128(L + GWL + fh * 160 + t * 16));
  u32x2 ex[3];
#pragma unroll
  for (int i = 0; i < 3; ++i) {
    const f32x4 x4 = acc[i][4] * z[8];
    f32x4 d4 = acc[i][0] * z[0] + acc[i][1] * z[1] + acc[i][2] * z[2] + acc[i][3] * z[3];
    f32x4 e4 = acc[i][5] * z[4] + acc[i][6] * z[5] + acc[i][7] * z[6] + acc[i][8] * z[7];
    f32x2 d2 = f32x2{d4[0], d4[1]} + f32x2{d4[2], d4[3]} + f32x2{x4[0], x4[1]};
    f32x2 e2 = f32x2{e4[0], e4[1]} + f32x2{e4[2], e4[3]} + f32x2{x4[2], x4[3]};
    const float r = __builtin_amdgcn_rcpf(d2[0] + d2[1] + EPS), s = __builtin_amdgcn_rcpf(e2[0] + e2[1] + EPS);
    const u32 p = rt0 + i < 128 ? po + i * (18 * 1024) : OOR;
    st_sc1(rs, p, pack2(acc[i][0] * r, acc[i][1] * r));
    st_sc1(rs, p + 1024, pack2(acc[i][2] * r, acc[i][3] * r));
    st_sc1(rs, p + 2048, pack2(acc[i][5] * s, acc[i][6] * s));
    st_sc1(rs, p + 3072, pack2(acc[i][7] * s, acc[i][8] * s));
    ex[i] = u32x2{pk(acc[i][4][0] * r, acc[i][4][1] * r), pk(acc[i][4][2] * s, acc[i][4][3] * s)};
    SCHED();
  }
#pragma unroll
  for (int k = 0; k < 2; ++k) {
    const int ib = k ? 2 : 1;
    auto sx = __builtin_amdgcn_permlane32_swap(ex[2 * k][0], ex[ib][0], false, false);
    auto sy = __builtin_amdgcn_permlane32_swap(ex[2 * k][1], ex[ib][1], false, false);
    const bool ok = rt0 + 2 * k + (lane >> 5) < 128 && (k == 0 || lane < 32);
    st_sc1(rs, ok ? pe + k * (36 * 1024) : OOR, u32x4{sx[0], sy[0], sx[1], sy[1]});
  }
}

struct cx2 { f32x2 r, i; };
DI cx2 cmul(cx2 a, cx2 b) { return cx2{a.r * b.r - a.i * b.i, a.r * b.i + a.i * b.r}; }
DI cx2 csq(cx2 a) { return cx2{a.r * a.r - a.i * a.i, (a.r + a.r) * a.i}; }
DI cx2 cpow(f32x2 lr, f32x2 rv, float n) {
  cx2 o;
#pragma unroll
  for (int m = 0; m < 2; ++m) {
    float mag = __builtin_amdgcn_exp2f(lr[m] * n), ph = __builtin_amdgcn_fractf(rv[m] * n);
    o.r[m] = mag * __builtin_amdgcn_cosf(ph);
    o.i[m] = mag * __builtin_amdgcn_sinf(ph);
  }
  return o;
}
constexpr int UB = 4608, PIT = 544, STG = UB, TBL = STG + 16 * PIT, WVB = TBL + 4368;
constexpr int TPE = TBL, TPD = 1216, TPO = TBL + TPD, F2 = TBL + 2368, F2S = 144, F3 = F2 + 4 * F2S, F3S = 176, FCP = 960;
static_assert(8 * WVB <= K1_LDS - 256 && WVB % 16 == 0, "lds");
struct SsmTab { u32 v[9][64][4]; };
constexpr SsmTab make_tab() {
  SsmTab t{};
  for (int l = 0; l < 64; ++l) {
    const int fr = l & 15, fh = l >> 4;
    const int lo[5] = {3, 6, 5, 4, 3}, hi[5] = {5, 10, 11, 12, 4};
    for (int k = 0; k < 5; ++k)
      for (int jj = 0; jj < 8; ++jj) {
        const int xx = fr + 16 - 8 * fh - jj;
        if (xx >= lo[k] && xx <= hi[k]) t.v[k][l][jj >> 1] |= (k == 4 ? 0xffffu : 0x3f80u) << (16 * (jj & 1));
      }
    for (int j = 0; j < 4; ++j)
      t.v[5][l][j] = fh < 2 ? F2 + (2 * (fr & 1) + (j & 1)) * F2S + (47 - 16 * fh - 8 * (j >> 1) - (fr >> 1)) * 2
                            : F3 + (fr & 1) * F3S + (63 - 32 * (fh - 2) - 8 * j - (fr >> 1)) * 2;
    t.v[6][l][0] = TBL + 1080 - 2 * (128 * (fh >> 1) + 4 * (fh & 1) + 8 * fr);
    t.v[6][l][1] = TBL + 558 - 16 * (l & 31);
    t.v[6][l][2] = TBL + (l == 31 ? 60 : l == 63 ? 574 : 1088);
    t.v[6][l][3] = l < 32 ? l * 16 : l < 36 ? TPE + (l - 32) * 16 : l < 40 ? TPE + 1088 + (l - 36) * 16
                   : l < 44 ? TPO + (l - 40) * 16 : l < 48 ? TPO + 1088 + (l - 44) * 16 : F2;
    t.v[7][l][0] = fr * 32 + fh * 16;
    t.v[7][l][1] = ((fr & 1) ? TPO + 2 : TPE) + (16 - fr + 8 * fh) * 2;
    t.v[7][l][2] = F2 + ((fr & 1) ? 0 : FCP + 2) + (15 - fr + 4 * fh) * 2;
    t.v[7][l][3] = STG + fr * PIT + fh * 16;
    t.v[8][l][0] = (fh & 1) * 512 + (fr >> 2) * 128 + (fh >> 1) * 64 + (fr & 3) * 16;
  }
  return t;
}
__device__ const SsmTab ssm_tab = make_tab();

DI void role_ssm(lp L, const char* x, const u32* logA, const u32* Aim, const u32* Cre, const u32* Cim, const u16* logdt,
                 const u16* Dsk, char* ws, int cb, int wave, int lane STARG) {
  const int chn = cb * 8 + wave;
  const u32x4* tab = (const u32x4*)ssm_tab.v + lane;
  u32 pa, pi, pcr, pci, pdt, pds;
  u32x4 o5, o6, o7;
  asm volatile("global_load_dword %0, %9, %12\n\tglobal_load_dword %1, %9, %13\n\tglobal_load_ushort %4, %10, %16\n\t"
               "global_load_dword %2, %9, %14\n\tglobal_load_dword %3, %9, %15\n\tglobal_load_ushort %5, %10, %17\n\t"
               "global_load_dwordx4 %6, %11, %18\n\tglobal_load_dwordx4 %7, %11, %18 offset:1024\n\tglobal_load_dwordx4 %8, %11, %18 offset:2048"
               : "=&v"(pa), "=&v"(pi), "=&v"(pcr), "=&v"(pci), "=&v"(pdt), "=&v"(pds), "=&v"(o5), "=&v"(o6), "=&v"(o7)
               : "v"((chn * 64 + lane) * 4), "v"(chn * 2), "v"(lane * 16), "s"(logA), "s"(Aim), "s"(Cre), "s"(Cim), "s"(logdt), "s"(Dsk),
                 "s"(ssm_tab.v[5])
               : "memory");
  lp wv = L + wave * WVB;
  const int tid = wave * 64 + lane;
  w128(wv + F2 + lane * 16, u32x4{0, 0, 0, 0});
  if (lane < 56) w128(wv + F2 + 1024 + lane * 16, u32x4{0, 0, 0, 0});
  u32x4 ur[4];
  const char* xc = x + cb * 16;
  const u32 uo = 4 * tid * XROW;
  auto ldu = [&](int i) { SCHED(); ur[i] = *(const u32x4*)(xc + uo + i * XROW); SCHED(); };
  BAR();
  ldu(0);
  waitv<1>();
  asm volatile("" : "+v"(pa), "+v"(pi), "+v"(pcr), "+v"(pci), "+v"(pdt), "+v"(pds), "+v"(o5), "+v"(o6), "+v"(o7));
  const float ldt = bf2f(pdt), dsk = bf2f(pds);
  w128(wv + o6[3], u32x4{0, 0, 0, 0});
  lp fw[4] = {wv + o5[0], wv + o5[1], wv + o5[2], wv + o5[3]};
  lp e0 = wv + o6[0], e1 = wv + o6[1], e2 = wv + o6[2];
  lp ua = wv + o7[0], ta = wv + o7[1], t2 = wv + o7[2], fa = wv + o7[3], pw = wv + STG + lane * 8;
  CFENCE();
  STAMP(1);
  const float dt = rbf(__expf(ldt));
  f32x2 lr = {-rbf(__expf(bf2f(pa))) * dt, -rbf(__expf(bfhi(pa))) * dt};
  f32x2 rv = {bf2f(pi) * dt, bfhi(pi) * dt};
  lr *= L2E; rv *= 0.15915494309f;
  const cx2 w1 = cpow(lr, rv, 1.f), wb = cpow(lr, rv, 128.f);
  cx2 w8 = csq(csq(csq(w1)));
  w8.i = -w8.i;
  const cx2 cc = {f32x2{bf2f(pcr), bfhi(pcr)} * (2.f * dt), f32x2{bf2f(pci), bfhi(pci)} * (2.f * dt)};
  ldu(1);
  STAMP(2);
  auto put = [&](int row, cx2 v) { w64(pw + row * PIT, u32x2{pk(v.r[0], v.i[0]), pk(v.r[1], v.i[1])}); };
  auto get = [&](u32x4 (&f)[8]) {
    CFENCE();
#pragma unroll
    for (int s = 0; s < 8; ++s) f[s] = r128(fa + s * 64);
    CFENCE();
    SCHED();
  };
  const f32x4 zero = {0.f, 0.f, 0.f, 0.f};
  u32x4 yf[8], xf[8];
  {
    cx2 y = {f32x2{1.f, 1.f}, f32x2{0.f, 0.f}};
    put(0, y); y = w8; put(1, y);
#pragma unroll
    for (int r = 2; r < 16; ++r) { y = cmul(y, w8); put(r, y); if (r == 7) ldu(2); }
    get(yf);
    ldu(3);
  }
  STAMP(9);
  cx2 e[4];
  {
    cx2 v = cc, u8 = cmul(cc, wb);
#pragma unroll
    for (int a = 0; a < 8; ++a) {
      v = cmul(v, w1); u8 = cmul(u8, w1);
      put(a, v); put(8 + a, u8);
      if (a & 1) e[a >> 1] = u8;
    }
    get(xf);
  }
  STAMP(5);
  u32x4 xg[8], xh[8];
#pragma unroll
  for (int n = 0; n < 8; ++n) { e[n & 3] = cmul(e[n & 3], wb); put(n, e[n & 3]); }
  put(8, cc);
  get(xg);
#pragma unroll
  for (int b = 0; b < 4; ++b) {
    e[1] = cmul(e[1], wb); e[3] = cmul(e[3], wb);
    put(2 * b, e[1]); put(2 * b + 1, e[3]);
  }
#pragma unroll
  for (int n = 0; n < 8; ++n) { e[3] = cmul(e[3], wb); put(8 + n, e[3]); }
  get(xh);
  f32x4 k0 = zero, k1 = zero, k2 = zero;
#pragma unroll
  for (int n = 0; n < 8; ++n) { k0 = mfma(xf[n], yf[n], k0); k1 = mfma(xg[n], yf[n], k1); }
  SCHED();
#pragma unroll
  for (int n = 0; n < 8; ++n) k2 = mfma(xh[n], yf[n], k2);
  const u32 d32 = pk(k0[3], k0[2]);
  {
    const u32 d10 = pk(k0[1], k0[0]), d21 = pk(k0[2], k0[1]);
    w64(e0, u32x2{d32, d10});
    w16(e0 + TPD + 2, d32); w32(e0 + TPD + 4, d21); w16(e0 + TPD + 8, d10 >> 16);
  }
  {
    const u32 c33 = pk(k1[3], k1[3]), c32 = pk(k1[3], k1[2]), c11 = pk(k1[1], k1[1]), c10 = pk(k1[1], k1[0]), c21 = pk(k1[2], k1[1]);
    if (lane < 32) {
      w128(e1 + TPD + 2, u32x4{c33, c32, c11, c10});
      w16(e1, c33); w32(e1 + 2, c33); w32(e1 + 6, c21); w32(e1 + 10, c11); w16(e1 + 14, c10 >> 16);
    }
    if (lane == 31 || lane == 32 || lane == 63) {
      const u32 v = lane == 31 ? c33 : lane == 63 ? d32 : pk(k1[0] + dsk, 0.f);
      w16(e2, v); w16(e2 + TPD + 2, v);
    }
  }
  STAMP(3);
  u32x4 BX[5];
#pragma unroll
  for (int k = 0; k < 5; ++k) BX[k] = tab[k * 64];
  const u32 bo = ((const u32*)tab)[8 * 256];
  {
    const u32 f01 = pk(k2[0], k2[1]), f23 = pk(k2[2], k2[3]);
#pragma unroll
    for (int j = 0; j < 4; ++j) { const u32 v = (j < 2 ? f01 : f23) >> (16 * (j & 1)); w16(fw[j], v); w16(fw[j] + FCP + 2, v); }
  }
  const u32 k1024 = __builtin_amdgcn_readlane(pk(k2[3], k2[3]), 31);
  CFENCE();
  u32x4 T[10];
#pragma unroll
  for (int n = 0; n < 20; ++n) {
    const int k = n >> 1, hf = n & 1;
    const int o = k < 3 ? hf * F2S + 64 - 32 * k : k < 6 ? (2 + hf) * F2S + 64 - 32 * (k - 3) : 4 * F2S + hf * F3S + 96 - 32 * (k - 6);
    T[k][2 * hf] = r32(t2 + o); T[k][2 * hf + 1] = r32(t2 + o + 4);
  }
  const u32x4 F32 = r4x32(ta);
  STAMP(4);
#pragma unroll
  for (int c = 0; c < 4; ++c) {
    w64(L + (2 * c) * WVB + 512 + tid * 8, u32x2{__builtin_amdgcn_perm(ur[1][c], ur[0][c], 0x05040100), __builtin_amdgcn_perm(ur[3][c], ur[2][c], 0x05040100)});
    w64(L + (2 * c + 1) * WVB + 512 + tid * 8, u32x2{__builtin_amdgcn_perm(ur[1][c], ur[0][c], 0x07060302), __builtin_amdgcn_perm(ur[3][c], ur[2][c], 0x07060302)});
  }
  WAITL0();
  BAR();
  STAMP(6);
  u32x4 U[2][8], Fa[2], Fb[2];
  auto rdn = [&](int d0, int n) {
    const int q = d0 & 1;
    if (n == 0) { Fa[q][0] = r32(ta + 1024 - d0 * 64); Fa[q][1] = r32(ta + 1028 - d0 * 64); }
    else if (n == 1) { Fa[q][2] = r32(ta + 1032 - d0 * 64); Fa[q][3] = r32(ta + 1036 - d0 * 64); }
    else if (n < 10) U[q][n - 2] = r128(ua + 480 + (n - 2) * 512 - d0 * 64);
    else if (n == 10) { Fb[q][0] = r32(ta + 512 - d0 * 64); Fb[q][1] = r32(ta + 516 - d0 * 64); }
    else { Fb[q][2] = r32(ta + 520 - d0 * 64); Fb[q][3] = r32(ta + 524 - d0 * 64); }
  };
#pragma unroll
  for (int n = 0; n < 12; ++n) rdn(7, n);
  SCHED();
  u32x4 W1[6];
  f32x4 acc[8];
#pragma unroll
  for (int d0 = 7; d0 >= 0; --d0) {
    const int q = d0 & 1;
#pragma unroll
    for (int m = 0; m < 15; ++m) {
      if (d0 < 7 && m == 0) __builtin_amdgcn_s_waitcnt(0xC67F);
      if (d0 < 7 && m == 4) __builtin_amdgcn_s_waitcnt(0xC47F);
      if (m >= 8) acc[m - 7] = mfma(Fb[q], U[q][m - 8], acc[m - 7]);
      else acc[m] = mfma(Fa[q], U[q][m], d0 == 7 ? zero : acc[m]);
      SCHED();
      if (d0 > 0) { if (m < 12) rdn(d0 - 1, m); }
      else if (m < 6) W1[m] = r128(ua + 464 + m * 512);
      SCHED();
    }
  }
  STAMP(7);
  {
    const u32x4 FX = BX[4] & k1024;
    f32x4 s[6];
    u32x4 SA, SB, SC;
    auto box = [&](int b) {
      s[0] = mfma(U[0][b], BX[0], zero); s[1] = mfma(U[0][b], BX[1], zero);
      s[2] = mfma(W1[b], BX[0], zero); s[3] = mfma(W1[b], BX[1], zero);
      if (b < 4) { s[4] = mfma(U[0][b], BX[2], zero); s[5] = mfma(W1[b], BX[3], zero); }
    };
    box(0);
#pragma unroll
    for (int b = 0; b < 6; ++b) {
      acc[b + 2] = mfma(F32, U[0][b], acc[b + 2]);
      if (b < 4) acc[b + 4] = mfma(FX, U[0][b], acc[b + 4]);
      SA = pack2(s[0], s[1]); SB = pack2(s[2], s[3]);
      if (b < 4) SC = pack2(s[4], s[5]);
      if (b < 5) box(b + 1);
#pragma unroll
      for (int d = 0; d < 3; ++d)
        if (b + 2 + d < 8) acc[b + 2 + d] = mfma(SA, T[d], acc[b + 2 + d]);
#pragma unroll
      for (int d = 0; d < 3; ++d)
        if (b + 2 + d < 8) acc[b + 2 + d] = mfma(SB, T[3 + d], acc[b + 2 + d]);
      if (b < 4) {
#pragma unroll
        for (int d = 0; d < 4; ++d)
          if (b + 4 + d < 8) acc[b + 4 + d] = mfma(SC, T[6 + d], acc[b + 4 + d]);
      }
    }
#pragma unroll
    for (int i = 0; i < 76; ++i) { __builtin_amdgcn_sched_group_barrier(0x8, 1, 0); __builtin_amdgcn_sched_group_barrier(0x2, 1, 0); }
  }
  STAMP(8);
  const rsrc_t rs = wsrsrc(ws);
  const u32 dst = WS_BT + chn * 4096 + bo;
#pragma unroll
  for (int i = 0; i < 8; i += 2) {
    auto sx = __builtin_amdgcn_permlane16_swap(pk(acc[i][0], acc[i][1]), pk(acc[i + 1][0], acc[i + 1][1]), false, false);
    auto sy = __builtin_amdgcn_permlane16_swap(pk(acc[i][2], acc[i][3]), pk(acc[i + 1][2], acc[i + 1][3]), false, false);
    st_sc1(rs, dst + i * 512, u32x4{sx[0], sy[0], sx[1], sy[1]});
  }
}

DI void touch(const void* sbase, u32 voff) {
  asm volatile("s_mov_b32 m0, %2\n\ts_nop 0\n\tglobal_load_lds_dword %0, %1" ::"v"(voff), "s"(sbase), "s"(K1_LDS - 256) : "memory");
}
extern "C" __global__ __launch_bounds__(512, 2) void gm_k1(const char* x, const char* w, const char* wo, const u32* logA, const u32* Aim,
                                             const u32* Cre, const u32* Cim, const u16* logdt, const u16* Dsk, const char* gw, char* ws, const char* out) {
  extern __shared__ __attribute__((aligned(16))) char smem[];
  lp L = (lp)smem;
  STDECL;
  STAMP(0);
  const int xcd = blockIdx.x & 7, loc = (int)(blockIdx.x >> 3) - 1;
  const int wave = __builtin_amdgcn_readfirstlane(threadIdx.x >> 6), lane = threadIdx.x & 63;
  if (__builtin_expect(loc < 0, 0)) {
    const u64 pad0 = __builtin_amdgcn_s_memrealtime();
    const char* pc; u32 back, len;
    asm volatile("s_getpc_b64 %0\n1:\n\ts_mov_b32 %1, 1b-gm_k1\n\ts_mov_b32 %2, gm_code_end-gm_k1" : "=s"(pc), "=s"(back), "=s"(len));
    pc -= back;
    touch(pc, min((u32)threadIdx.x * 128, len));
    if (wave == 6) { touch(ws + WS_EP, lane * 128); touch(ws + WS_EP, min(8192 + lane * 128, 176 * 64 - 4)); }
    if (wave == 7) { touch(ws, min(lane * 131072, WS_END - 4)); touch(out, min(lane * 65536, SEQ * XROW - 4)); }
    const char* wsrc = wo + (xcd & 1) * (16 * 18 * XROW);
#pragma unroll
    for (int i = 0; i < 5; ++i) touch(wsrc + i * 65536, threadIdx.x * 128);
    u32x4 gv[9];
#pragma unroll
    for (int i = 0; i < 9; ++i) gv[i] = *(const u32x4*)(gw + i * 128 + (lane & 7) * 16);
    float res = 0.f;
#pragma unroll
    for (int p = 0; p < 4; ++p) {
      const char* xp = x + (u32)(xcd * 256 + wave * 32 + (lane >> 3) * 4 + p) * XROW + (lane & 7) * 16;
      u32x4 xv[9];
#pragma unroll
      for (int i = 0; i < 9; ++i) xv[i] = *(const u32x4*)(xp + i * 128);
      float a = 0.f;
#pragma unroll
      for (int i = 0; i < 9; ++i)
#pragma unroll
        for (int d = 0; d < 4; ++d) {
          const u32 xa = xv[i][d], ga = gv[i][d];
          a = __builtin_amdgcn_fdot2_f32_bf16(__builtin_bit_cast(bf16x2, xa), __builtin_bit_cast(bf16x2, ga), a, false);
        }
      a += __builtin_bit_cast(float, __builtin_amdgcn_update_dpp(0, __builtin_bit_cast(int, a), 0xb1, 0xf, 0xf, false));
      a += __builtin_bit_cast(float, __builtin_amdgcn_update_dpp(0, __builtin_bit_cast(int, a), 0x4e, 0xf, 0xf, false));
      a += __builtin_bit_cast(float, __builtin_amdgcn_update_dpp(0, __builtin_bit_cast(int, a), 0x141, 0xf, 0xf, false));
      res = (lane & 3) == p ? a : res;
    }
    res = rbf(__builtin_amdgcn_rcpf(1.f + __builtin_amdgcn_exp2f(-L2E * rbf(res))));
    res = __int_as_float(__builtin_amdgcn_ds_bpermute(((lane >> 2) * 8 + (lane & 3)) * 4, __float_as_int(res)));
    st32_sc1(wsrsrc(ws), lane < 32 ? WS_G + (xcd * 256 + wave * 32 + lane) * 4 : OOR, __float_as_uint(res));
    while (__builtin_amdgcn_s_memrealtime() - pad0 < PAD1) __builtin_amdgcn_s_sleep(4);
    return;
  }
  const bool isq = loc >= 25;
  const int Q = xcd >> 1, l = loc - 25;
  const int rq = ((0x8530 >> (4 * Q)) & 15) + (l >> 1), rk = Q * 4 + (loc >> 2);
  const int cq = (xcd & 1) * 2 + (l & 1), ck = (xcd & 1) * 4 + (loc & 3);
  const int sq = 3 * (l >> 1) + 4 * (l & 1) + 1, sk = 2 * ((loc >> 2) + (loc & 3));
  int rbi = isq ? rq : rk, cg = isq ? cq : ck, c0 = isq ? sq : sk;
  c0 = c0 >= 9 ? c0 - 9 : c0;
  asm volatile("" : "+s"(rbi), "+s"(cg), "+s"(c0));
  PINS(x); PINS(w); PINS(wo); PINS(logA); PINS(Aim); PINS(Cre); PINS(Cim); PINS(logdt); PINS(Dsk); PINS(gw); PINS(ws);
  if (Q == 1 && l >= 4) return;
  if (__builtin_expect((loc < 16 || isq) && wave < 4, 1)) {
    role_load(L, x, w, wo, ws, isq, rbi, cg, c0, wave, lane STPASS);
  } else if (loc < 16) {
    role_kv(L, ws, rbi, cg, wave - 4, lane STPASS);
  } else if (isq) {
    role_q(L, ws, rbi, cg, wave - 4, lane STPASS);
  } else {
    role_ssm(L, x, logA, Aim, Cre, Cim, logdt, Dsk, ws, xcd * 9 + loc - 16, wave, lane STPASS);
  }
  STFLUSH(0);
}

constexpr int K2S = 27648, K2B = 5 * K2S, K2G = K2B + 10240, SP = 2;
template <int NR>
DI void k2_compute(lp L, char* out, int rt0, int r0, int cb, int ch, int lane STARG) {
  const int fr = lane & 15, fh = lane >> 4;
  const int col = 32 * ch + 2 * fr;
  lp B0 = L + fh * 1024 + ((col ^ (col >> 4)) << 4), B1 = L + fh * 1024 + (((col + 1) ^ (col >> 4)) << 4);
  lp A = L + 12288 + r0 * 3072 + lane * 16;
  lp Be = L + SP * K2S + fh * 256 + col * 4, Ae = L + SP * K2S + 12288 + r0 * 3072 + (lane & 31) * 16 + (lane >> 5) * 8;
  PIN(B0); PIN(B1); PIN(A); PIN(Be); PIN(Ae); PINS(out);
  f32x4 acc[NR][2], cst[NR][2], g[NR];
#pragma unroll
  for (int sl = 0; sl < 6; ++sl) {
    const int so = (sl == 5 ? 0 : sl) * K2S;
    lp b0 = B0, b1 = B1, a0 = A;
    if (so > 32768) { b0 += 2 * K2S; b1 += 2 * K2S; a0 += 2 * K2S; }
    const int o = so > 32768 ? so - 2 * K2S : so;
    BAR();
    STAMP(1 + sl);
    u32x4 a[3][NR], b[3][2];
#pragma unroll
    for (int s = 0; s < 3; ++s) {
      if (sl == SP && s > 0) {
#pragma unroll
        for (int t = 0; t < 2; ++t) { lp e = Be + s * 4096 + t * 4; b[s][t] = u32x4{r32(e), r32(e + 1024), r32(e + 2048), r32(e + 3072)}; }
#pragma unroll
        for (int i = 0; i < NR; ++i) {
          const u32x2 lo = r64(Ae + i * 3072 + s * 1024), hi = r64(Ae + i * 3072 + s * 1024 + 512);
          a[s][i] = u32x4{lo[0], lo[1], hi[0], hi[1]};
        }
      } else {
        b[s][0] = r128(b0 + o + s * 4096);
#pragma unroll
        for (int i = 0; i < NR; ++i) a[s][i] = r128(a0 + o + i * 3072 + s * 1024);
        SCHED();
        b[s][1] = r128(b1 + o + s * 4096);
      }
      SCHED();
    }
#pragma unroll
    for (int s = 0; s < 3; ++s)
#pragma unroll
      for (int t = 0; t < 2; ++t)
#pragma unroll
        for (int i = 0; i < NR; ++i) acc[i][t] = mfma(a[s][i], b[s][t], sl + s ? acc[i][t] : f32x4{0.f, 0.f, 0.f, 0.f});
    SCHED();
    if (sl == 3) {
#pragma unroll
      for (int i = 0; i < NR; ++i) {
        g[i] = __builtin_bit_cast(f32x4, r128(L + K2G + (r0 + i) * 64 + fh * 16));
#pragma unroll
        for (int t = 0; t < 2; ++t) {
          const u32x2 bv = r64(L + K2B + (col + t) * 160 + ((fh >> 1) * 5 + r0 + i) * 16 + (fh & 1) * 8);
          cst[i][t] = f32x4{bf2f(bv[0]), bfhi(bv[0]), bf2f(bv[1]), bfhi(bv[1])} * (1.f - g[i]);
          PIN(cst[i][t]);
        }
      }
    }
  }
  STAMP(7);
  const rsrc_t ro = __builtin_amdgcn_make_buffer_rsrc(out, 0, SEQ * XROW, 0x00020000);
  const u32 dst = ((rt0 + r0) * 16 + fh * 4) * XROW + cb * 128 + col * 2;
#pragma unroll
  for (int i = 0; i < NR; ++i) {
    const f32x4 u = acc[i][0] * g[i] + cst[i][0], v = acc[i][1] * g[i] + cst[i][1];
#pragma unroll
    for (int j = 0; j < 4; ++j) __builtin_amdgcn_raw_buffer_store_b32(pk(u[j], v[j]), ro, dst + (16 * i + j) * XROW, 0, AUX_OUT);
  }
}

extern "C" __global__ __launch_bounds__(512, 2) void gm_k2(const char* ws, char* out) {
  extern __shared__ __attribute__((aligned(16))) char smem[];
  lp L = (lp)smem;
  STDECL;
  STAMP(0);
  const int xcd = blockIdx.x & 7, id = xcd * 29 + (xcd < 2 ? xcd : 2) + (blockIdx.x >> 3);
  const int rb = (id * 7282) >> 16, cb = id - rb * 9;
  const int wave = __builtin_amdgcn_readfirstlane(threadIdx.x >> 6), lane = threadIdx.x & 63;
  if (__builtin_expect(wave < 4, 1)) {
    const u64 pad0 = __builtin_amdgcn_s_memrealtime();
    __builtin_amdgcn_s_setprio(2);
    const u32 ord = (u32)(0x104321043210ull >> (4 * ((cb + 2 * rb) & 7)));
    const u32 lo = lane * 16;
    const int t1 = min(5 * rb + wave, 127), t4 = min(5 * rb + 4, 127);
    const char* mb = ws + (WS_M + cb * 73728 + wave * 3072);
    const char* p1 = ws + (WS_PQ + t1 * 18432);
    const char* p4 = ws + (WS_PQ + t4 * 18432 + wave * 768);
    auto issue = [&](int sl) {
      const u32 gq = sl == SP ? 5 : (ord >> (4 * (sl - (sl > SP)))) & 15;
      const u32 lb = (sl == 5 ? 0 : sl) * K2S;
      glds3(mb + gq * 12288, lb + wave * 3072, lo);
      glds3(p1 + gq * 3072, lb + 12288 + wave * 3072, lo);
      asm volatile("s_mov_b32 m0, %2\n\ts_mov_b32 exec_hi, 0xffff\n\tglobal_load_lds_dwordx4 %0, %1\n\ts_mov_b32 exec_hi, -1"
                   ::"v"(lo), "s"(p4 + gq * 3072), "s"(lb + 24576 + wave * 768) : "memory");
    };
    issue(0); issue(1);
    waitv<7>(); BAR();
    issue(2);
    u32 bo[3];
#pragma unroll
    for (int i = 0; i < 3; ++i) {
      const int f = wave * 160 + i * 64 + (i < 2 ? lane : lane & 31);
      const int ir = (f * 6554) >> 16, pc = f - ir * 10, hh = pc >= 5;
      const int rt = min(5 * rb + pc - 5 * hh, 127);
      bo[i] = ir * 4096 + (rt >> 2) * 128 + hh * 64 + (rt & 3) * 16;
    }
    waitv<7>(); BAR();
    {
      const char* bt = ws + (WS_BT + cb * (64 * 4096));
      glds(bt, bo[0], K2B + wave * 2560); glds(bt, bo[1], K2B + wave * 2560 + 1024);
      glds_lo(bt, bo[2], K2B + wave * 2560 + 2048);
      if (wave == 0 && lane < 20) glds(ws + WS_G, min(rb * 320 + lane * 16, SEQ * 4 - 16), K2G);
    }
    issue(3); waitv<10>(); BAR();
    issue(4); waitv<7>(); BAR();
    issue(5); waitv<7>(); BAR();
    waitv<0>(); BAR();
    __builtin_amdgcn_s_setprio(0);
    while (__builtin_amdgcn_s_memrealtime() - pad0 < PAD2) __builtin_amdgcn_s_sleep(4);
  } else if (wave < 6) {
    k2_compute<3>(L, out, 5 * rb, 0, cb, wave & 1, lane STPASS);
  } else {
    k2_compute<2>(L, out, 5 * rb, 3, cb, wave & 1, lane STPASS);
  }
  STFLUSH(1);
}

extern "C" __device__ __attribute__((used, noinline)) void gm_code_end() {}

extern "C" void kernel_launch(void* const* d_in, const int* in_sizes, int n_in, void* d_out, int out_size, void* d_ws,
                              size_t ws_size, hipStream_t stream) {
  (void)hipFuncSetAttribute((const void*)gm_k1, hipFuncAttributeMaxDynamicSharedMemorySize, K1_LDS);
  (void)hipFuncSetAttribute((const void*)gm_k2, hipFuncAttributeMaxDynamicSharedMemorySize, K2_LDS);
  gm_k1<<<256, 512, K1_LDS, stream>>>((const char*)d_in[0], (const char*)d_in[1], (const char*)d_in[2], (const u32*)d_in[4],
                                   (const u32*)d_in[5], (const u32*)d_in[6], (const u32*)d_in[7], (const u16*)d_in[8],
                                   (const u16*)d_in[9], (const char*)d_in[10], (char*)d_ws, (const char*)d_out);
  gm_k2<<<234, 512, K2_LDS, stream>>>((const char*)d_ws, (char*)d_out);
}
```

```cpp
#include <hip/hip_runtime.h>
#include <stdint.h>

typedef unsigned int u32;
typedef unsigned short u16;
typedef unsigned long long u64;
typedef __attribute__((ext_vector_type(2))) unsigned u32x2;
typedef __attribute__((ext_vector_type(4))) unsigned u32x4;
typedef __attribute__((ext_vector_type(2))) float f32x2;
typedef __attribute__((ext_vector_type(4))) float f32x4;
typedef __attribute__((ext_vector_type(4))) short s16x4;
typedef __attribute__((ext_vector_type(2))) __bf16 bf16x2;
typedef __attribute__((ext_vector_type(8))) __bf16 bf16x8;
#define LAS __attribute__((address_space(3)))
#define GAS __attribute__((address_space(1)))
#define DI __device__ __forceinline__
typedef LAS char* lp;

constexpr int SEQ = 2048, DM = 576;
constexpr int XROW = DM * 2;
constexpr int WROW = 3 * DM * 2;
constexpr int WS_PQ = 0;
constexpr int WS_M = WS_PQ + 128 * 18 * 1024;
constexpr int WS_BT = WS_M + 36 * 18 * 1024;
#define BTP 4224
constexpr int WS_G = WS_BT + DM * BTP;
constexpr int WS_MBKV = WS_G + SEQ * 4;
constexpr int MBKV_SZ = 16 * 640;
constexpr int WS_MBZ = WS_MBKV + 128 * MBKV_SZ;
constexpr int MBZ_SZ = 2 * 16 * 384;
constexpr int WS_EP = WS_MBZ + 44 * MBZ_SZ;
constexpr int WS_END = WS_EP + 176 * 128;
constexpr int K1_LDS = 162304;
constexpr int K2_LDS = 5 * 27648 + 10240 + 320 + 64;
#define PF_BLOCKS 1
#define CSTN 13
#define K2LA 3
#define ROT1 1
#define AUX_OUT 16

DI float bf2f(u32 v) { return __uint_as_float(v << 16); }
DI float bfhi(u32 v) { return __uint_as_float(v & 0xffff0000u); }
DI u32 pk(float a, float b) { bf16x2 v = {(__bf16)a, (__bf16)b}; return __builtin_bit_cast(u32, v); }
DI float rbf(float a) { return bf2f(pk(a, a) & 0xffffu); }
DI f32x4 mfma(u32x4 a, u32x4 b, f32x4 c) {
  return __builtin_amdgcn_mfma_f32_16x16x32_bf16(__builtin_bit_cast(bf16x8, a), __builtin_bit_cast(bf16x8, b), c, 0, 0, 0);
}
DI u32x4 r128(lp a) { return *(LAS u32x4*)a; }
DI u32x2 r64(lp a) { return *(LAS u32x2*)a; }
DI u32 r32(lp a) { return *(LAS u32*)a; }
DI void w128(lp a, u32x4 v) { *(LAS u32x4*)a = v; }
DI void w64(lp a, u32x2 v) { *(LAS u32x2*)a = v; }
DI void w32(lp a, u32 v) { *(LAS u32*)a = v; }
DI void w16(lp a, u32 v) { *(LAS u16*)a = (u16)v; }
DI u32x2 rtr(lp a) { return __builtin_bit_cast(u32x2, __builtin_amdgcn_ds_read_tr16_b64_v4i16((LAS s16x4*)a)); }
DI u32x4 rtr2(lp a0, lp a1) { u32x2 l = rtr(a0), h = rtr(a1); return u32x4{l[0], l[1], h[0], h[1]}; }
DI u32x4 r4x32(lp a) { return u32x4{r32(a), r32(a + 4), r32(a + 8), r32(a + 12)}; }
DI u32x4 r2x2(lp a, lp b) { return u32x4{r32(a), r32(a + 4), r32(b), r32(b + 4)}; }
DI void glds(const void* sbase, u32 voff, u32 lds) {
  asm volatile("s_mov_b32 m0, %2\n\ts_nop 0\n\tglobal_load_lds_dwordx4 %0, %1" ::"v"(voff), "s"(sbase), "s"(lds) : "memory");
}
DI void glds4(const void* sbase, u32 lds, u32 v0, u32 v1, u32 v2, u32 v3) {
  asm volatile("s_mov_b32 m0, %5\n\ts_nop 0\n\tglobal_load_lds_dwordx4 %0, %4\n\tglobal_load_lds_dwordx4 %1, %4 offset:1024\n\t"
               "global_load_lds_dwordx4 %2, %4 offset:2048\n\tglobal_load_lds_dwordx4 %3, %4 offset:3072"
               ::"v"(v0), "v"(v1), "v"(v2), "v"(v3), "s"(sbase), "s"(lds) : "memory");
}
DI void glds2(const void* sbase, u32 lds, u32 v0, u32 v1) {
  asm volatile("s_mov_b32 m0, %3\n\ts_nop 0\n\tglobal_load_lds_dwordx4 %0, %2\n\tglobal_load_lds_dwordx4 %1, %2 offset:1024"
               ::"v"(v0), "v"(v1), "s"(sbase), "s"(lds) : "memory");
}
DI void glds3(const void* sbase, u32 lds, u32 v) {
  asm volatile("s_mov_b32 m0, %2\n\ts_nop 0\n\tglobal_load_lds_dwordx4 %0, %1\n\tglobal_load_lds_dwordx4 %0, %1 offset:1024\n\t"
               "global_load_lds_dwordx4 %0, %1 offset:2048" ::"v"(v), "s"(sbase), "s"(lds) : "memory");
}
DI void glds_lo(const void* sbase, u32 voff, u32 lds) {
  asm volatile("s_mov_b32 m0, %2\n\ts_mov_b32 exec_hi, 0\n\tglobal_load_lds_dwordx4 %0, %1\n\ts_mov_b32 exec_hi, -1"
               ::"v"(voff), "s"(sbase), "s"(lds) : "memory");
}
DI void touch(const void* sbase, u32 voff) {
  asm volatile("s_mov_b32 m0, %2\n\ts_nop 0\n\tglobal_load_lds_dword %0, %1 sc1" ::"v"(voff), "s"(sbase), "s"(K1_LDS - 256) : "memory");
}
template <int N> DI void waitv() { asm volatile("s_waitcnt vmcnt(%0)" ::"i"(N) : "memory"); }
#define CFENCE() asm volatile("" ::: "memory")
#define WAITL0() do { __builtin_amdgcn_s_waitcnt(0xC07F); CFENCE(); } while (0)
#define BAR() do { CFENCE(); __builtin_amdgcn_s_barrier(); CFENCE(); } while (0)
#define SCHED() __builtin_amdgcn_sched_barrier(0)
#define PIN(x) asm volatile("" : "+v"(x))
#define PINS(x) asm volatile("" ::"s"(x))
constexpr float EPS = 1e-6f, L2E = 1.44269504089f;
DI f32x2 phi2(float a0, float a1) {
  f32x2 m = f32x2{a0, a1} * L2E - EPS * L2E;
  f32x2 e = {__builtin_amdgcn_fmed3f(__builtin_amdgcn_exp2f(m[0]), 0.f, 1.f), __builtin_amdgcn_fmed3f(__builtin_amdgcn_exp2f(m[1]), 0.f, 1.f)};
  const int ei = __builtin_bit_cast(int, EPS);
  f32x2 x = {__int_as_float(max(__float_as_int(a0), ei)), __int_as_float(max(__float_as_int(a1), ei))};
  return x + e;
}
DI f32x4 phi4(f32x4 a) { f32x2 l = phi2(a[0], a[1]), h = phi2(a[2], a[3]); return f32x4{l[0], l[1], h[0], h[1]}; }
DI float rowsum16(float v) {
  v += __builtin_bit_cast(float, __builtin_amdgcn_update_dpp(0, __builtin_bit_cast(int, v), 0x128, 0xf, 0xf, false));
  v += __builtin_bit_cast(float, __builtin_amdgcn_update_dpp(0, __builtin_bit_cast(int, v), 0x124, 0xf, 0xf, false));
  v += __builtin_bit_cast(float, __builtin_amdgcn_update_dpp(0, __builtin_bit_cast(int, v), 0x122, 0xf, 0xf, false));
  v += __builtin_bit_cast(float, __builtin_amdgcn_update_dpp(0, __builtin_bit_cast(int, v), 0x121, 0xf, 0xf, false));
  return v;
}
DI float prodsum(float a, float b, float c, float d) {
  auto s0 = __builtin_amdgcn_permlane32_swap(__float_as_uint(a), __float_as_uint(c), false, false);
  auto s1 = __builtin_amdgcn_permlane32_swap(__float_as_uint(b), __float_as_uint(d), false, false);
  const float ac = __uint_as_float(s0[0]) + __uint_as_float(s0[1]), bd = __uint_as_float(s1[0]) + __uint_as_float(s1[1]);
  auto s2 = __builtin_amdgcn_permlane16_swap(__float_as_uint(ac), __float_as_uint(bd), false, false);
  float v = __uint_as_float(s2[0]) + __uint_as_float(s2[1]);
  v += __builtin_bit_cast(float, __builtin_amdgcn_update_dpp(0, __builtin_bit_cast(int, v), 0x128, 0xf, 0xf, false));
  v += __builtin_bit_cast(float, __builtin_amdgcn_update_dpp(0, __builtin_bit_cast(int, v), 0x124, 0xf, 0xf, false));
  return v;
}
DI float prodsum(u32x4 m) { return prodsum(bf2f(m[0]), bfhi(m[0]), bf2f(m[2]), bfhi(m[2])); }
DI u32x4 pack2(f32x4 a, f32x4 b) { return u32x4{pk(a[0], a[1]), pk(a[2], a[3]), pk(b[0], b[1]), pk(b[2], b[3])}; }
typedef __amdgpu_buffer_rsrc_t rsrc_t;
DI rsrc_t wsrsrc(char* ws) { return __builtin_amdgcn_make_buffer_rsrc(ws, 0, WS_END, 0x00020000); }
DI void st_sc1(rsrc_t r, u32 off, u32x4 v) { __builtin_amdgcn_raw_buffer_store_b128(v, r, off, 0, 16); }
DI u32x4 ld_sc1(rsrc_t r, u32 off) { return __builtin_amdgcn_raw_buffer_load_b128(r, off, 0, 16); }
DI void st32_sc1(rsrc_t r, u32 off, u32 v) { __builtin_amdgcn_raw_buffer_store_b32(v, r, off, 0, 16); }
DI u32 ld_ep(char* ws, int i) { return __hip_atomic_load((GAS u32*)(ws + WS_EP) + 32 * i, __ATOMIC_RELAXED, __HIP_MEMORY_SCOPE_AGENT); }
DI void st_ep(rsrc_t r, int i, int lane, u32 v) { if (lane < 8) st_sc1(r, WS_EP + 128 * i + 16 * lane, u32x4{v, v, v, v}); }
constexpr u32 OOR = 0x7ffffff0u;
constexpr u32 SPIN_CAP = 1u << 15;
#define SENT_SLEEP 4
#define POLL_SLEEP 4
#define STDECL
#define STARG
#define STPASS
#define STAMP(i)
#define STFLUSH(k)

constexpr int XR_KV = 4 * 18432, XR_Q = 3 * 18432;
constexpr int GWL = XR_KV + 4 * 16384;
constexpr int WOI = GWL + 1280;
constexpr int XCH = XR_KV + 16384, KVA = 18432;

template <int ROLE>
DI void gemm_main(lp L, int rg, int lane, f32x4 (&acc)[ROLE ? 3 : 2][9] STARG) {
  constexpr int RT = ROLE ? 3 : 2, XB = RT * 8192, XRING = ROLE ? XR_Q : XR_KV, NS = ROLE ? 3 : 4;
  const int fr = lane & 15, fh = lane >> 4;
  const int q4 = (lane >> 2) & 3, p4 = lane & 3;
  const int ax0 = XRING + (rg * RT * 16 + fr) * 128 + ((fh ^ ((fr >> 1) & 7)) << 4), ax1 = ax0 ^ 64;
  const int r0 = (8 * fh + q4 + 4 * (fh & 1)) * 288, r1 = (8 * fh + q4 + 4 * ((fh & 1) ^ 1)) * 288;
  const int cm = ROLE ? 72 * p4 : 8 * p4, cx = 72 * p4 + 32;
  u32x4 a0[RT], a1[RT], b0[9], b1[9];
  lp W0 = L + r0 + cm, W1 = L + r1 + cm, E0 = L + r0 + cx, E1 = L + r1 + cx, X0 = L + ax0, X1 = L + ax1;
  PIN(E0); PIN(E1);
  auto step = [&](bool domma, u32x4 (&a)[RT], u32x4 (&b)[9], int slot, int s, u32x4 (&an)[RT], u32x4 (&bn)[9]) {
    auto rd = [&](int n) {
      if (n < RT) { an[n] = r128((s ? X1 : X0) + slot * XB + n * 2048); return; }
      n -= RT;
      if (n >= 18) return;
      const int t = n >> 1, so = slot * 18432 + s * 9216;
      const int to = (ROLE ? 8 * t : 72 * (t >> 1) + 40 * (t & 1)) + so;
      const u32x2 v = (ROLE || t < 8) ? rtr((n & 1 ? W1 : W0) + to) : rtr((n & 1 ? E1 : E0) + so);
      bn[t][2 * (n & 1)] = v[0]; bn[t][2 * (n & 1) + 1] = v[1];
    };
    const int EX = ROLE ? 0 : (s ? 7 : 2);
#pragma unroll
    for (int m = 0; m < 9 * RT; ++m) {
      const int t = m / RT, i = m % RT;
      if (domma) acc[i][t] = ROLE ? mfma(b[t], a[i], acc[i][t]) : mfma(a[i], b[t], acc[i][t]);
      SCHED();
      if (m < 2 * EX) { rd(m + (m + 1) / 2); if (!(m & 1)) rd(m + m / 2 + 1); } else rd(m + EX);
      SCHED();
    }
  };
#pragma unroll
  for (int i = 0; i < RT; ++i)
#pragma unroll
    for (int t = 0; t < 9; ++t) { const float v = (ROLE || t < 4 || (t == 8 && fr < 8)) ? EPS : 0.f; acc[i][t] = f32x4{v, v, v, v}; PIN(acc[i][t]); }
  STAMP(1);
  BAR();
  STAMP(2);
  step(false, a1, b1, 0, 0, a0, b0);
#pragma unroll 1
  for (int it = 0; it < (ROLE ? 3 : 2); ++it) {
#pragma unroll
    for (int sl = 0; sl < NS; ++sl) {
      step(true, a0, b0, sl, 1, a1, b1);
      WAITL0();
      BAR();
      SCHED();
      step(true, a1, b1, sl == NS - 1 ? 0 : sl + 1, 0, a0, b0);
    }
  }
  if (ROLE == 0) {
    step(true, a0, b0, 0, 1, a1, b1);
#pragma unroll
    for (int t = 0; t < 9; ++t)
#pragma unroll
      for (int i = 0; i < RT; ++i) acc[i][t] = mfma(a1[i], b1[t], acc[i][t]);
  }
}

DI void kv_consume(lp L, rsrc_t rs, int rb, int g, u32 tme, int wave, int lane STARG) {
  const int chead = 4 * g + (rb & 3), crole = rb >> 2;
  if (crole == 3) return;
  const int fr = lane & 15, fh = lane >> 4;
  const bool ext = crole == 2;
  u32x4 wf[6];
  {
    const int codd = chead & 1, q4 = (lane >> 2) & 3, p4 = lane & 3;
    int s0 = 8 * fh + q4, s1 = s0 + 4;
    s0 = s0 > 17 ? 17 : s0; s1 = s1 > 17 ? 17 : s1;
    s0 += 2 * codd; s1 += 2 * codd;
    s0 = s0 >= 18 ? s0 - 18 : s0; s1 = s1 >= 18 ? s1 - 18 : s1;
    lp p0 = L + WOI + s0 * XROW + wave * 64 + p4 * 8, p1 = L + WOI + s1 * XROW + wave * 64 + p4 * 8;
#pragma unroll
    for (int i = 0; i < 6; ++i) wf[i] = rtr2(p0 + (i >> 1) * 512 + (i & 1) * 32, p1 + (i >> 1) * 512 + (i & 1) * 32);
  }
  const int un = 2 * (chead >> 3) + (chead & 1), hh = (chead >> 1) & 3;
  const int col = (wave & 1) * 32 + (lane & 31);
  u32 mo = WS_M + (wave >> 1) * 73728 + (ext ? 65536 + (un * 4 + hh) * 256 + col * 4 : (2 * un + crole) * 4096 + hh * 1024 + ((col ^ (col >> 4)) << 4));
  if (lane >= 32) mo = OOR;
  lp afp = L + KVA + fr * 64 + fh * 16;
  PIN(mo); PIN(afp);
  waitv<0>();
  STAMP(6);
  if (wave < (ext ? 5 : 8)) {
    const int it = 4 * wave + (lane & 3);
    const u32 off = WS_MBKV + (g * 16 + rb) * MBKV_SZ + (lane >> 2) * 640 + it * 16;
    const bool two = wave == 0 && !ext;
    const bool keep = fr < 4 && (!ext || (it < 18 && (fh >> 1) == (chead & 1)));
    lp d = L + KVA + (!keep ? 512 + lane * 2 : ext ? (fh & 1) * 64 + it * 2 : ((it >> 4) * 4 + fh) * 64 + (it & 15) * 2);
    lp d2 = L + KVA + (fr < 4 ? ((fr >> 1) * 4 + fh) * 64 + 32 + (fr & 1) * 2 : 512 + lane * 2);
    PIN(d); PIN(d2);
    u32x4 m, e = {0, tme, 0, tme};
    for (u32 spin = 0;; ++spin) {
      m = ld_sc1(rs, off);
      if (two) e = ld_sc1(rs, off + 512);
      bool ok = m[1] == tme && m[3] == tme && e[1] == tme && e[3] == tme;
      if (__all(ok) || spin > SPIN_CAP) break;
      __builtin_amdgcn_s_sleep(POLL_SLEEP);
    }
    STAMP(7);
    const float sm = prodsum(m);
    w16(d, pk(sm, sm));
    if (two) { const float se = prodsum(e); w16(d2, pk(se, se)); }
  }
  WAITL0();
  BAR();
  STAMP(8);
  const u32x4 af = r128(afp);
#pragma unroll
  for (int i = 0; i < 3; ++i) {
    if (i == 2 && wave >= 2) break;
    const f32x4 u = mfma(af, wf[2 * i], f32x4{0.f, 0.f, 0.f, 0.f}), v = mfma(af, wf[2 * i + 1], f32x4{0.f, 0.f, 0.f, 0.f});
    auto sx = __builtin_amdgcn_permlane16_swap(pk(u[0], u[1]), pk(v[0], v[1]), false, false);
    auto sy = __builtin_amdgcn_permlane16_swap(pk(u[2], u[3]), pk(v[2], v[3]), false, false);
    if (ext) st32_sc1(rs, mo + i * (4 * 73728), sx[0]);
    else st_sc1(rs, mo + i * (4 * 73728), u32x4{sx[0], sy[0], sx[1], sy[1]});
  }
  if (wave == 7) st_ep(rs, g * 16 + rb, lane, tme);
}

DI void role_load(lp L, const char* x, const char* w, const char* wo_g, char* ws, bool isq, int rbi, int cg, int c0,
                  int lw, int lane STARG) {
  __builtin_amdgcn_s_setprio(2);
  const bool six = isq && rbi < 10;
  const int XP = isq ? 6 : 4;
  const char* xr = x + rbi * (isq ? 192 * XROW : 128 * XROW);
  const u32 xl = (isq ? XR_Q : XR_KV) + lw * XP * 1024, XB = XP * 4096;
  u32 xo[4], wo[5];
  xo[0] = (8 * (six || !isq ? lw * XP : (lw * XP > 12 ? 12 : lw * XP)) + (lane >> 3)) * XROW + (((lane & 7) ^ (lane >> 4)) << 4);
  xo[1] = (xo[0] ^ 64) + 8192; xo[2] = xo[0] + 16384; xo[3] = xo[1] + 16384;
  auto issx = [&](int cidx, int slot) {
    const char* xb = xr + cidx * 128;
    glds4(xb, xl + slot * XB, xo[0], xo[1], xo[2], xo[3]);
    if (isq) glds2(xb + (six || lw < 2 ? 32 * XROW : 0), xl + slot * XB + 4096, xo[0], xo[1]);
  };
  auto issw = [&](int cidx, int slot) {
    const char* wb = w + cidx * (64 * WROW);
    const u32 lb = slot * 18432 + lw * 4608;
    glds4(wb, lb, wo[0], wo[1], wo[2], wo[3]);
    glds_lo(wb, wo[4], lb + 4096);
  };
  auto nxt = [](int c) { return c == 8 ? 0 : c + 1; };
  STAMP(3);
  issx(c0, 0);
  SCHED();
  {
    const int cb0 = (isq ? 288 * cg : 1152 + 144 * cg) + 16 * lw * WROW, cb1 = isq ? cb0 : cb0 + 1008;
#pragma unroll
    for (int i = 0; i < 5; ++i) {
      const int f = i < 4 ? 64 * i + lane : 256 + (lane & 31);
      const int lr = (f * 3641) >> 16, cc = f - lr * 18;
      wo[i] = (lr ^ ((lr >> 1) & 4)) * WROW + (cc < 9 ? cb0 : cb1) + 16 * cc - (i < 4 ? 1024 * i : 0);
    }
  }
  issw(c0, 0);
  STAMP(9);
  int c = nxt(c0);
  const int fr = lane & 15, fh = lane >> 4;
  const int me = isq ? 128 + cg * 11 + rbi : cg * 16 + rbi;
  const bool isz = lane < 48;
  const int jq = lane >> 2, cz = 128 + (cg >> 1) * 11 + (jq > 10 ? 10 : jq);
  const int ca = isz ? cz : cg * 16 + lw + ((lane >> 1) & 4), cb_ = isz ? cz : cg * 16 + lw + 8;
  u32 tme, ta, tb;
  if (isq) {
    issx(c, 1); issw(c, 1);
    c = nxt(c);
    int sx = 2;
    STAMP(1);
    waitv<11>();
    BAR();
    STAMP(2);
#pragma unroll 1
    for (int k = 0; k < 7; ++k) {
      issx(c, sx); issw(c, sx);
      c = nxt(c);
      sx = sx == 2 ? 0 : sx + 1;
      waitv<11>();
      BAR();
    }
    waitv<0>();
    tme = ld_ep(ws, me); ta = tb = 0;
    BAR();
  } else {
    issx(c, 1); issw(c, 1);
    c = nxt(c);
    const char* wb = w + c * (64 * WROW);
    const char* xb = xr + c * 128;
    asm volatile("" : "+s"(wb), "+s"(xb));
    STAMP(1);
    waitv<9>();
    BAR();
    STAMP(2);
    glds4(xb, xl + 2 * 16384, xo[0], xo[1], xo[2], xo[3]);
#pragma unroll
    for (int k = 0; k < 7; ++k) {
      const u32 lb = ((k + 2) & 3) * 18432 + lw * 4608;
      glds4(wb, lb, wo[0], wo[1], wo[2], wo[3]);
      glds_lo(wb, wo[4], lb + 4096);
      if (k < 6) {
        c = nxt(c);
        xb = xr + c * 128; wb = w + c * (64 * WROW);
        asm volatile("" : "+s"(wb), "+s"(xb));
        glds4(xb, xl + ((k + 3) & 3) * 16384, xo[0], xo[1], xo[2], xo[3]);
        waitv<13>();
      } else {
        waitv<9>();
      }
      BAR();
    }
    if (rbi < 12) {
      const char* src = wo_g + (4 * cg + (rbi & 3)) * (18 * XROW) + lw * 5184;
      glds4(src, WOI + lw * 5184, lane * 16, lane * 16, lane * 16, lane * 16);
      glds(src + 4096, lane * 16, WOI + lw * 5184 + 4096);
      if (lane < 4) glds(src + 5120, lane * 16, WOI + lw * 5184 + 5120);
      waitv<6>();
    } else {
      waitv<0>();
    }
    BAR();
    tme = ld_ep(ws, me); ta = ld_ep(ws, ca); tb = ld_ep(ws, cb_);
  }
  const rsrc_t rs = wsrsrc(ws);
  tme += 1; ta += 1; tb += 1;
  if (isq) {
    BAR();
    const int i0 = lw * 4 + (lane & 3);
    const u32 base = WS_MBZ + (me - 128) * MBZ_SZ + (lane >> 2) * 384;
    u32 o[3]; lp zw[3];
#pragma unroll
    for (int i = 0; i < 3; ++i) {
      const int it = i0 + 16 * i, gi = it >= 20, r = it - 20 * gi;
      o[i] = base + gi * (MBZ_SZ / 2) + r * 16;
      zw[i] = L + GWL + (r < 16 ? (2 * gi + (r >> 3)) * 160 + (r & 7) * 16 : (2 * gi + (r & 1)) * 160 + 128 + (r & 2) * 8) + fh * 4;
    }
    const u32 so = __builtin_amdgcn_readfirstlane(base) + (lw & 1) * (MBZ_SZ / 2) + ((4 * lw + rbi) & 15) * 384 + 324;
    for (u32 spin = 0; spin < SPIN_CAP; ++spin) {
      if (__builtin_amdgcn_readfirstlane(__builtin_amdgcn_raw_buffer_load_b32(rs, 0, so, 16)) == __builtin_amdgcn_readfirstlane(tme)) break;
      __builtin_amdgcn_s_sleep(SENT_SLEEP);
    }
    STAMP(6);
    u32x4 m[3];
    m[0] = m[1] = u32x4{0, tme + 1, 0, tme};
    m[2] = u32x4{0, lw < 2 ? tme + 1 : tme, 0, tme};
    for (u32 spin = 0;; ++spin) {
      bool ok = true;
#pragma unroll
      for (int i = 0; i < 3; ++i) { const u32x4 v = ld_sc1(rs, m[i][1] == tme && m[i][3] == tme ? OOR : o[i]); if (!(m[i][1] == tme && m[i][3] == tme)) m[i] = v; }
#pragma unroll
      for (int i = 0; i < 3; ++i) ok = ok && m[i][1] == tme && m[i][3] == tme;
      if (__all(ok) || spin > SPIN_CAP) break;
      __builtin_amdgcn_s_sleep(POLL_SLEEP);
    }
    STAMP(7);
#pragma unroll
    for (int i = 0; i < 3; ++i) {
      if (i == 2 && lw >= 2) break;
      const float sv = prodsum(m[i]);
      if (fr < 4) w32(zw[i], __float_as_uint(sv));
    }
    WAITL0();
    BAR();
    if (lw == 0) st_ep(rs, me, lane, tme);
    return;
  }
  __builtin_amdgcn_s_setprio(0);
  if (lw == 0) w128(L + KVA + lane * 16, u32x4{0, 0, 0, 0});
  const u32 zb = WS_MBZ + (cz - 128) * MBZ_SZ + (cg & 1) * (MBZ_SZ / 2) + rbi * 384 + (lane & 3) * 16;
  u32 oa = isz ? (jq < 11 ? zb + lw * 64 : OOR) : WS_MBKV + ca * MBKV_SZ + rbi * 640 + 512 + (lane & 7) * 16;
  u32 ob = isz ? (jq < 11 && lw < 2 ? zb + 256 + lw * 64 : OOR) : lane < 56 ? WS_MBKV + cb_ * MBKV_SZ + rbi * 640 + 256 + (lane & 7) * 16 : OOR;
  int sa = isz ? (lane & 3) * 64 : ((((lane >> 2) & 2) + ((lane >> 1) & 1)) * 16 + 8 + 2 * lw + (lane & 1)) * 4;
  int sb = isz ? (lane & 3) * 64 : ((lw >> 1) * 16 + 8 + 2 * lw + (lane & 1)) * 4;
  lp bk = L + XCH + lane * 16 + lw * 1024, bx = L + XCH + lane * 16 + 8192;
  const bool own = (fr >> 1) == 4 + lw;
  PIN(oa); PIN(ob); PIN(sa); PIN(sb); PIN(bk); PIN(bx); PIN(ta); PIN(tb); PIN(tme);
  waitv<0>();
  WAITL0();
  BAR();
  STAMP(4);
  {
    u32x4 kf[4], xf[4];
#pragma unroll
    for (int r = 0; r < 4; ++r) { xf[r] = r128(bx + r * 9216); kf[r] = r128(bk + r * 9216); }
    SCHED();
    f32x4 p2 = {0.f, 0.f, 0.f, 0.f}, p4 = p2;
#pragma unroll
    for (int r = 0; r < 4; ++r) {
      u32x4 xo;
#pragma unroll
      for (int d = 0; d < 4; ++d) xo[d] = own ? xf[r][d] : 0x3f803f80u;
      p2 = mfma(kf[r], xo, p2); p4 = mfma(xf[r], xo, p4);
    }
    const u32 a0 = __builtin_amdgcn_ds_bpermute(sa, pk(p2[0], p2[1])), a1 = __builtin_amdgcn_ds_bpermute(sa, pk(p2[2], p2[3]));
    const u32 b0 = __builtin_amdgcn_ds_bpermute(sb, pk(p4[0], p4[1])), b1 = __builtin_amdgcn_ds_bpermute(sb, pk(p4[2], p4[3]));
    st_sc1(rs, oa, u32x4{a0, ta, a1, ta});
    st_sc1(rs, ob, u32x4{b0, tb, b1, tb});
  }
  STAMP(5);
  kv_consume(L, rs, rbi, cg, tme, lw, lane STPASS);
}

DI void role_kv(lp L, char* ws, int rb, int g, int hi, int lane STARG) {
  const int fr = lane & 15, fh = lane >> 4;
  const rsrc_t rs = wsrsrc(ws);
  const int c0 = g * 16 + hi + (lane < 32 ? 0 : 4), c1 = g * 16 + hi + 8;
  u32 d0 = WS_MBKV + c0 * MBKV_SZ + rb * 640 + (lane & 31) * 16;
  u32 d1 = fh == (hi >> 1) ? WS_MBKV + c1 * MBKV_SZ + rb * 640 + fr * 16 : OOR;
  u32 e0 = WS_EP + c0 * 128;
  lp xw = L + XCH + hi * 9216 + lane * 16, bk = L + XCH + hi * 1024 + lane * 16, bx = L + XCH + 8192 + lane * 16;
  PIN(d0); PIN(d1); PIN(e0); PIN(xw); PIN(bk); PIN(bx);
  f32x4 acc[2][9];
  gemm_main<0>(L, hi, lane, acc STPASS);
  u32 t0 = __builtin_amdgcn_raw_buffer_load_b32(rs, e0, 0, 16), t1 = __builtin_amdgcn_raw_buffer_load_b32(rs, 0, WS_EP + c1 * 128, 16);
  u32 tme = __builtin_amdgcn_raw_buffer_load_b32(rs, 0, WS_EP + (g * 16 + rb) * 128, 16);
  STAMP(3);
  {
#pragma unroll
    for (int t = 0; t < 4; ++t) {
      w128(xw + t * 1024, pack2(phi4(acc[0][t]), phi4(acc[1][t])));
      w128(xw + 4096 + t * 1024, pack2(acc[0][4 + t], acc[1][4 + t]));
    }
    const u32x4 xp = pack2(phi4(acc[0][8]), phi4(acc[1][8])), xr = pack2(acc[0][8], acc[1][8]);
    w128(xw + 8192, fr < 8 ? xp : xr);
  }
  t0 += 1; t1 += 1; tme += 1;
  PIN(t0); PIN(t1); PIN(tme);
  WAITL0();
  BAR();
  STAMP(4);
  {
    u32x4 xf[4], vf[4], kf[4];
#pragma unroll
    for (int r = 0; r < 4; ++r) { kf[r] = r128(bk + r * 9216); vf[r] = r128(bk + 4096 + r * 9216); }
    SCHED();
#pragma unroll
    for (int r = 0; r < 4; ++r) xf[r] = r128(bx + r * 9216);
    SCHED();
    f32x4 p1 = {0.f, 0.f, 0.f, 0.f}, p3 = p1;
#pragma unroll
    for (int r = 0; r < 4; ++r) p1 = mfma(kf[r], vf[r], p1);
#pragma unroll
    for (int r = 0; r < 4; ++r) p3 = mfma(xf[r], vf[r], p3);
    st_sc1(rs, d0, u32x4{pk(p1[0], p1[1]), t0, pk(p1[2], p1[3]), t0});
    st_sc1(rs, d1, u32x4{pk(p3[0], p3[1]), t1, pk(p3[2], p3[3]), t1});
  }
  STAMP(5);
  kv_consume(L, rs, rb, g, tme, 4 + hi, lane STPASS);
}

DI void role_q(lp L, char* ws, int jq, int hg, int rg, int lane STARG) {
  const int fh = lane >> 4;
  const rsrc_t rs = wsrsrc(ws);
  f32x4 acc[3][9];
  gemm_main<1>(L, rg, lane, acc STPASS);
  STAMP(3);
#pragma unroll
  for (int i = 0; i < 3; ++i)
#pragma unroll
    for (int t = 0; t < 9; ++t) { acc[i][t] = phi4(acc[i][t]); PIN(acc[i][t]); }
  const int rt0 = jq * 12 + rg * 3;
  u32 po = WS_PQ + rt0 * (18 * 1024) + hg * 4096 + lane * 16;
  u32 pe = WS_PQ + (rt0 + (lane >> 5)) * (18 * 1024) + 16384 + hg * 512 + (lane & 31) * 16;
  PIN(po); PIN(pe);
  STAMP(7);
  BAR();
  STAMP(8);
  f32x4 z[9];
#pragma unroll
  for (int t = 0; t < 9; ++t) z[t] = __builtin_bit_cast(f32x4, r128(L + GWL + fh * 160 + t * 16));
  u32x2 ex[3];
#pragma unroll
  for (int i = 0; i < 3; ++i) {
    const f32x4 x4 = acc[i][4] * z[8];
    f32x4 d4 = acc[i][0] * z[0] + acc[i][1] * z[1] + acc[i][2] * z[2] + acc[i][3] * z[3];
    f32x4 e4 = acc[i][5] * z[4] + acc[i][6] * z[5] + acc[i][7] * z[6] + acc[i][8] * z[7];
    f32x2 d2 = f32x2{d4[0], d4[1]} + f32x2{d4[2], d4[3]} + f32x2{x4[0], x4[1]};
    f32x2 e2 = f32x2{e4[0], e4[1]} + f32x2{e4[2], e4[3]} + f32x2{x4[2], x4[3]};
    const float r = __builtin_amdgcn_rcpf(d2[0] + d2[1] + EPS), s = __builtin_amdgcn_rcpf(e2[0] + e2[1] + EPS);
    const u32 p = rt0 + i < 128 ? po + i * (18 * 1024) : OOR;
    st_sc1(rs, p, pack2(acc[i][0] * r, acc[i][1] * r));
    st_sc1(rs, p + 1024, pack2(acc[i][2] * r, acc[i][3] * r));
    st_sc1(rs, p + 2048, pack2(acc[i][5] * s, acc[i][6] * s));
    st_sc1(rs, p + 3072, pack2(acc[i][7] * s, acc[i][8] * s));
    ex[i] = u32x2{pk(acc[i][4][0] * r, acc[i][4][1] * r), pk(acc[i][4][2] * s, acc[i][4][3] * s)};
    SCHED();
  }
#pragma unroll
  for (int k = 0; k < 2; ++k) {
    const int ib = k ? 2 : 1;
    auto sx = __builtin_amdgcn_permlane32_swap(ex[2 * k][0], ex[ib][0], false, false);
    auto sy = __builtin_amdgcn_permlane32_swap(ex[2 * k][1], ex[ib][1], false, false);
    const bool ok = rt0 + 2 * k + (lane >> 5) < 128 && (k == 0 || lane < 32);
    st_sc1(rs, ok ? pe + k * (36 * 1024) : OOR, u32x4{sx[0], sy[0], sx[1], sy[1]});
  }
}

struct cx2 { f32x2 r, i; };
DI cx2 cmul(cx2 a, cx2 b) { return cx2{a.r * b.r - a.i * b.i, a.r * b.i + a.i * b.r}; }
DI cx2 csq(cx2 a) { return cx2{a.r * a.r - a.i * a.i, (a.r + a.r) * a.i}; }
DI cx2 cpow(f32x2 lr, f32x2 rv, float n) {
  cx2 o;
#pragma unroll
  for (int m = 0; m < 2; ++m) {
    float mag = __builtin_amdgcn_exp2f(lr[m] * n), ph = __builtin_amdgcn_fractf(rv[m] * n);
    o.r[m] = mag * __builtin_amdgcn_cosf(ph);
    o.i[m] = mag * __builtin_amdgcn_sinf(ph);
  }
  return o;
}
constexpr int UB = 4608, PIT = 544, STG = UB, TBL = STG + 16 * PIT, WVB = TBL + 4368;
constexpr int TPE = TBL, TPD = 1216, TPO = TBL + TPD, F2 = TBL + 2368, F2S = 144, F3 = F2 + 4 * F2S, F3S = 176, FCP = 960;
static_assert(8 * WVB <= K1_LDS - 256 && WVB % 16 == 0, "lds");
struct SsmTab { u32 v[8][64][4]; };
constexpr SsmTab make_tab() {
  SsmTab t{};
  for (int l = 0; l < 64; ++l) {
    const int fr = l & 15, fh = l >> 4;
    const int lo[5] = {3, 6, 5, 4, 3}, hi[5] = {5, 10, 11, 12, 4};
    for (int k = 0; k < 5; ++k)
      for (int jj = 0; jj < 8; ++jj) {
        const int xx = fr + 16 - 8 * fh - jj;
        if (xx >= lo[k] && xx <= hi[k]) t.v[k][l][jj >> 1] |= (k == 4 ? 0xffffu : 0x3f80u) << (16 * (jj & 1));
      }
    for (int j = 0; j < 4; ++j)
      t.v[5][l][j] = fh < 2 ? F2 + (2 * (fr & 1) + (j & 1)) * F2S + (47 - 16 * fh - 8 * (j >> 1) - (fr >> 1)) * 2
                            : F3 + (fr & 1) * F3S + (63 - 32 * (fh - 2) - 8 * j - (fr >> 1)) * 2;
    t.v[6][l][0] = TBL + 1080 - 2 * (128 * (fh >> 1) + 4 * (fh & 1) + 8 * fr);
    t.v[6][l][1] = l < 32 ? TBL + 558 - 16 * l : STG + 14 + 16 * l;
    t.v[6][l][2] = l == 31 ? TBL + 60 : l == 63 ? TBL + 574 : l == 32 ? TBL + 1088 : STG + 4 * l;
    t.v[6][l][3] = l < 32 ? l * 16 : l < 36 ? TPE + (l - 32) * 16 : l < 40 ? TPE + 1088 + (l - 36) * 16
                   : l < 44 ? TPO + (l - 40) * 16 : l < 48 ? TPO + 1088 + (l - 44) * 16 : F2;
    t.v[7][l][0] = fr * 32 + fh * 16;
    t.v[7][l][1] = ((fr & 1) ? TPO + 2 : TPE) + (16 - fr + 8 * fh) * 2;
    t.v[7][l][2] = F2 + ((fr & 1) ? 0 : FCP + 2) + (15 - fr + 4 * fh) * 2;
    t.v[7][l][3] = STG + fr * PIT + fh * 16;
  }
  return t;
}
__device__ const SsmTab ssm_tab = make_tab();

DI void role_ssm(lp L, const char* x, const u32* logA, const u32* Aim, const u32* Cre, const u32* Cim, const u16* logdt,
                 const u16* Dsk, char* ws, int cb, int wave, int lane STARG) {
  const int chn = cb * 8 + wave;
  const GAS u32x4* tab = (const GAS u32x4*)ssm_tab.v[2] + lane;
  PIN(tab);
  u32 pa, pi, pcr, pci, pdt, pds;
  u32x4 o5, o6, o7;
  asm volatile("global_load_dword %0, %9, %12\n\tglobal_load_dword %1, %9, %13\n\tglobal_load_ushort %4, %10, %16\n\t"
               "global_load_dword %2, %9, %14\n\tglobal_load_dword %3, %9, %15\n\tglobal_load_ushort %5, %10, %17\n\t"
               "global_load_dwordx4 %6, %11, %18\n\tglobal_load_dwordx4 %7, %11, %18 offset:1024\n\tglobal_load_dwordx4 %8, %11, %18 offset:2048"
               : "=&v"(pa), "=&v"(pi), "=&v"(pcr), "=&v"(pci), "=&v"(pdt), "=&v"(pds), "=&v"(o5), "=&v"(o6), "=&v"(o7)
               : "v"((chn * 64 + lane) * 4), "v"(chn * 2), "v"(lane * 16), "s"(logA), "s"(Aim), "s"(Cre), "s"(Cim), "s"(logdt), "s"(Dsk),
                 "s"(ssm_tab.v[5])
               : "memory");
  lp wv = L + wave * WVB;
  const int tid = wave * 64 + lane;
  w128(wv + F2 + lane * 16, u32x4{0, 0, 0, 0});
  if (lane < 56) w128(wv + F2 + 1024 + lane * 16, u32x4{0, 0, 0, 0});
  u32x4 ur[4];
  const char* xc = x + cb * 16;
  const u32 uo = 4 * tid * XROW;
  auto ldu = [&](int i) { SCHED(); ur[i] = *(const u32x4*)(xc + uo + i * XROW); SCHED(); };
  BAR();
  ldu(0);
  waitv<1>();
  asm volatile("" : "+v"(pa), "+v"(pi), "+v"(pcr), "+v"(pci), "+v"(pdt), "+v"(pds), "+v"(o5), "+v"(o6), "+v"(o7));
  const float ldt = bf2f(pdt), dsk = bf2f(pds);
  w128(wv + o6[3], u32x4{0, 0, 0, 0});
  lp fw[4] = {wv + o5[0], wv + o5[1], wv + o5[2], wv + o5[3]};
  lp e0 = wv + o6[0], e1 = wv + o6[1], e2 = wv + o6[2];
  lp ua = wv + o7[0], ta = wv + o7[1], t2 = wv + o7[2], fa = wv + o7[3], pw = wv + STG + lane * 8;
  CFENCE();
  STAMP(1);
  const float dt = rbf(__expf(ldt));
  f32x2 lr = {-rbf(__expf(bf2f(pa))) * dt, -rbf(__expf(bfhi(pa))) * dt};
  f32x2 rv = {bf2f(pi) * dt, bfhi(pi) * dt};
  lr *= L2E; rv *= 0.15915494309f;
  const cx2 w1 = cpow(lr, rv, 1.f), wb = cpow(lr, rv, 128.f);
  cx2 w8 = csq(csq(csq(w1)));
  w8.i = -w8.i;
  const cx2 cc = {f32x2{bf2f(pcr), bfhi(pcr)} * (2.f * dt), f32x2{bf2f(pci), bfhi(pci)} * (2.f * dt)};
  ldu(1);
  STAMP(2);
  auto put = [&](int row, cx2 v) { w64(pw + row * PIT, u32x2{pk(v.r[0], v.i[0]), pk(v.r[1], v.i[1])}); };
  auto get = [&](u32x4 (&f)[8]) {
    CFENCE();
#pragma unroll
    for (int s = 0; s < 8; ++s) f[s] = r128(fa + s * 64);
    CFENCE();
    SCHED();
  };
  const f32x4 zero = {0.f, 0.f, 0.f, 0.f};
  u32x4 yf[8], xf[8];
  {
    cx2 y = {f32x2{1.f, 1.f}, f32x2{0.f, 0.f}};
    put(0, y); y = w8; put(1, y);
#pragma unroll
    for (int r = 2; r < 16; ++r) { y = cmul(y, w8); put(r, y); if (r == 7) ldu(2); }
    get(yf);
    ldu(3);
  }
  STAMP(9);
  cx2 e[4];
  {
    cx2 v = cc, u8 = cmul(cc, wb);
#pragma unroll
    for (int a = 0; a < 8; ++a) {
      v = cmul(v, w1); u8 = cmul(u8, w1);
      put(a, v); put(8 + a, u8);
      if (a & 1) e[a >> 1] = u8;
    }
    get(xf);
  }
  STAMP(5);
  u32x4 xg[8], xh[8];
#pragma unroll
  for (int n = 0; n < 8; ++n) { e[n & 3] = cmul(e[n & 3], wb); put(n, e[n & 3]); }
  put(8, cc);
  get(xg);
#pragma unroll
  for (int b = 0; b < 4; ++b) {
    e[1] = cmul(e[1], wb); e[3] = cmul(e[3], wb);
    put(2 * b, e[1]); put(2 * b + 1, e[3]);
  }
#pragma unroll
  for (int n = 0; n < 8; ++n) { e[3] = cmul(e[3], wb); put(8 + n, e[3]); }
  get(xh);
  f32x4 k0 = zero, k1 = zero, k2 = zero;
#pragma unroll
  for (int n = 0; n < 8; ++n) { k0 = mfma(xf[n], yf[n], k0); k1 = mfma(xg[n], yf[n], k1); }
  SCHED();
#pragma unroll
  for (int n = 0; n < 8; ++n) k2 = mfma(xh[n], yf[n], k2);
  const u32 d32 = pk(k0[3], k0[2]);
  {
    const u32 d10 = pk(k0[1], k0[0]), d21 = pk(k0[2], k0[1]);
    w64(e0, u32x2{d32, d10});
    w16(e0 + TPD + 2, d32); CFENCE(); w32(e0 + TPD + 4, d21); CFENCE(); w16(e0 + TPD + 8, d10 >> 16);
  }
  {
    const u32 c33 = pk(k1[3], k1[3]), c32 = pk(k1[3], k1[2]), c11 = pk(k1[1], k1[1]), c10 = pk(k1[1], k1[0]), c21 = pk(k1[2], k1[1]);
    w128(e1 + TPD + 2, u32x4{c33, c32, c11, c10});
    w16(e1, c33); CFENCE(); w32(e1 + 2, c33); CFENCE(); w32(e1 + 6, c21); CFENCE(); w32(e1 + 10, c11); CFENCE(); w16(e1 + 14, c10 >> 16);
    u32 v = pk(k1[0] + dsk, 0.f);
    PIN(v);
    v = lane < 32 ? c33 : lane < 48 ? v : d32;
    w16(e2, v); w16(e2 + TPD + 2, v);
  }
  STAMP(3);
  u32x4 BX[5];
#pragma unroll
  for (int k = 0; k < 5; ++k) BX[k] = tab[(k - 2) * 64];
  const u32 bo = (lane & 16) * 32 + (lane & 12) * 32 + (lane & 32) * 2 + (lane & 3) * 16;
  {
    const u32 f01 = pk(k2[0], k2[1]), f23 = pk(k2[2], k2[3]);
#pragma unroll
    for (int j = 0; j < 4; ++j) { const u32 v = (j < 2 ? f01 : f23) >> (16 * (j & 1)); w16(fw[j], v); w16(fw[j] + FCP + 2, v); }
  }
  const u32 k1024 = __builtin_amdgcn_readlane(pk(k2[3], k2[3]), 31);
  CFENCE();
  u32x4 T[10];
#pragma unroll
  for (int n = 0; n < 20; ++n) {
    const int k = n >> 1, hf = n & 1;
    const int o = k < 3 ? hf * F2S + 64 - 32 * k : k < 6 ? (2 + hf) * F2S + 64 - 32 * (k - 3) : 4 * F2S + hf * F3S + 96 - 32 * (k - 6);
    T[k][2 * hf] = r32(t2 + o); T[k][2 * hf + 1] = r32(t2 + o + 4);
  }
  const u32x4 F32 = r4x32(ta);
  STAMP(4);
#pragma unroll
  for (int c = 0; c < 4; ++c) {
    w64(L + (2 * c) * WVB + 512 + tid * 8, u32x2{__builtin_amdgcn_perm(ur[1][c], ur[0][c], 0x05040100), __builtin_amdgcn_perm(ur[3][c], ur[2][c], 0x05040100)});
    w64(L + (2 * c + 1) * WVB + 512 + tid * 8, u32x2{__builtin_amdgcn_perm(ur[1][c], ur[0][c], 0x07060302), __builtin_amdgcn_perm(ur[3][c], ur[2][c], 0x07060302)});
  }
  WAITL0();
  BAR();
  STAMP(6);
  u32x4 U[2][8], Fa[2], Fb[2];
  auto rdn = [&](int d0, int n) {
    const int q = d0 & 1;
    if (n == 0) { Fa[q][0] = r32(ta + 1024 - d0 * 64); Fa[q][1] = r32(ta + 1028 - d0 * 64); }
    else if (n == 1) { Fa[q][2] = r32(ta + 1032 - d0 * 64); Fa[q][3] = r32(ta + 1036 - d0 * 64); }
    else if (n < 10) U[q][n - 2] = r128(ua + 480 + (n - 2) * 512 - d0 * 64);
    else if (n == 10) { Fb[q][0] = r32(ta + 512 - d0 * 64); Fb[q][1] = r32(ta + 516 - d0 * 64); }
    else { Fb[q][2] = r32(ta + 520 - d0 * 64); Fb[q][3] = r32(ta + 524 - d0 * 64); }
  };
#pragma unroll
  for (int n = 0; n < 12; ++n) rdn(7, n);
  SCHED();
  u32x4 W1[6];
  f32x4 acc[8];
#pragma unroll
  for (int d0 = 7; d0 >= 0; --d0) {
    const int q = d0 & 1;
#pragma unroll
    for (int m = 0; m < 15; ++m) {
      if (d0 < 7 && m == 0) __builtin_amdgcn_s_waitcnt(0xC67F);
      if (d0 < 7 && m == 4) __builtin_amdgcn_s_waitcnt(0xC47F);
      if (m >= 8) acc[m - 7] = mfma(Fb[q], U[q][m - 8], acc[m - 7]);
      else acc[m] = mfma(Fa[q], U[q][m], d0 == 7 ? zero : acc[m]);
      SCHED();
      if (d0 > 0) { if (m < 12) rdn(d0 - 1, m); }
      else if (m < 6) W1[m] = r128(ua + 464 + m * 512);
      SCHED();
    }
  }
  STAMP(7);
  {
    const u32x4 FX = BX[4] & k1024;
    f32x4 s[6];
    u32x4 SA, SB, SC;
    auto box = [&](int b) {
      s[0] = mfma(U[0][b], BX[0], zero); s[1] = mfma(U[0][b], BX[1], zero);
      s[2] = mfma(W1[b], BX[0], zero); s[3] = mfma(W1[b], BX[1], zero);
      if (b < 4) { s[4] = mfma(U[0][b], BX[2], zero); s[5] = mfma(W1[b], BX[3], zero); }
    };
    box(0);
#pragma unroll
    for (int b = 0; b < 6; ++b) {
      acc[b + 2] = mfma(F32, U[0][b], acc[b + 2]);
      if (b < 4) acc[b + 4] = mfma(FX, U[0][b], acc[b + 4]);
      SA = pack2(s[0], s[1]); SB = pack2(s[2], s[3]);
      if (b < 4) SC = pack2(s[4], s[5]);
      if (b < 5) box(b + 1);
#pragma unroll
      for (int d = 0; d < 3; ++d)
        if (b + 2 + d < 8) acc[b + 2 + d] = mfma(SA, T[d], acc[b + 2 + d]);
#pragma unroll
      for (int d = 0; d < 3; ++d)
        if (b + 2 + d < 8) acc[b + 2 + d] = mfma(SB, T[3 + d], acc[b + 2 + d]);
      if (b < 4) {
#pragma unroll
        for (int d = 0; d < 4; ++d)
          if (b + 4 + d < 8) acc[b + 4 + d] = mfma(SC, T[6 + d], acc[b + 4 + d]);
      }
    }
#pragma unroll
    for (int i = 0; i < 76; ++i) { __builtin_amdgcn_sched_group_barrier(0x8, 1, 0); __builtin_amdgcn_sched_group_barrier(0x2, 1, 0); }
  }
  STAMP(8);
  const rsrc_t rs = wsrsrc(ws);
  const u32 dst = WS_BT + chn * BTP + bo;
#pragma unroll
  for (int i = 0; i < 8; i += 2) {
    auto sx = __builtin_amdgcn_permlane16_swap(pk(acc[i][0], acc[i][1]), pk(acc[i + 1][0], acc[i + 1][1]), false, false);
    auto sy = __builtin_amdgcn_permlane16_swap(pk(acc[i][2], acc[i][3]), pk(acc[i + 1][2], acc[i + 1][3]), false, false);
    st_sc1(rs, dst + i * 512, u32x4{sx[0], sy[0], sx[1], sy[1]});
  }
}

extern "C" __global__ __launch_bounds__(512, 2) void gm_k1(const char* x, const char* w, const char* wo, const u32* logA, const u32* Aim,
                                             const u32* Cre, const u32* Cim, const u16* logdt, const u16* Dsk, const char* gw, char* ws, const char* out) {
  extern __shared__ __attribute__((aligned(16))) char smem[];
  lp L = (lp)smem;
  STDECL;
  STAMP(0);
  const int xcd = blockIdx.x & 7, loc = (int)(blockIdx.x >> 3) - 1;
  const int wave = __builtin_amdgcn_readfirstlane(threadIdx.x >> 6), lane = threadIdx.x & 63;
  if (__builtin_expect(loc < 0, 0)) {
    const char* pc; u32 back, len;
    asm volatile("s_getpc_b64 %0\n1:\n\ts_mov_b32 %1, 1b-gm_k1\n\ts_mov_b32 %2, gm_code_end-gm_k1" : "=s"(pc), "=s"(back), "=s"(len));
    pc -= back;
    touch(pc, min((u32)threadIdx.x * 128, len));
    if (wave < 3) touch(ws + WS_EP, min(threadIdx.x * 128, 175u * 128));
    if (wave == 7) { touch(ws, min(lane * 131072, WS_END - 4)); touch(out, min(lane * 65536, SEQ * XROW - 4)); }
    const char* wsrc = wo + (xcd & 1) * (16 * 18 * XROW);
    u32x4 gv[9];
#pragma unroll
    for (int i = 0; i < 9; ++i) gv[i] = *(const u32x4*)(gw + i * 128 + (lane & 7) * 16);
    float res = 0.f;
#pragma unroll
    for (int p = 0; p < 4; ++p) {
      const char* xp = x + (u32)(xcd * 256 + wave * 32 + (lane >> 3) * 4 + p) * XROW + (lane & 7) * 16;
      u32x4 xv[9];
#pragma unroll
      for (int i = 0; i < 9; ++i) xv[i] = *(const u32x4*)(xp + i * 128);
      float a = 0.f;
#pragma unroll
      for (int i = 0; i < 9; ++i)
#pragma unroll
        for (int d = 0; d < 4; ++d) {
          const u32 xa = xv[i][d], ga = gv[i][d];
          a = __builtin_amdgcn_fdot2_f32_bf16(__builtin_bit_cast(bf16x2, xa), __builtin_bit_cast(bf16x2, ga), a, false);
        }
      a += __builtin_bit_cast(float, __builtin_amdgcn_update_dpp(0, __builtin_bit_cast(int, a), 0xb1, 0xf, 0xf, false));
      a += __builtin_bit_cast(float, __builtin_amdgcn_update_dpp(0, __builtin_bit_cast(int, a), 0x4e, 0xf, 0xf, false));
      a += __builtin_bit_cast(float, __builtin_amdgcn_update_dpp(0, __builtin_bit_cast(int, a), 0x141, 0xf, 0xf, false));
      res = (lane & 3) == p ? a : res;
    }
    res = rbf(__builtin_amdgcn_rcpf(1.f + __builtin_amdgcn_exp2f(-L2E * rbf(res))));
    res = __int_as_float(__builtin_amdgcn_ds_bpermute(((lane >> 2) * 8 + (lane & 3)) * 4, __float_as_int(res)));
    st32_sc1(wsrsrc(ws), lane < 32 ? WS_G + (xcd * 256 + wave * 32 + lane) * 4 : OOR, __float_as_uint(res));
    return;
  }
  const bool isq = loc >= 25;
  const int Q = xcd >> 1, l = loc - 25;
  const int rq = ((0x8530 >> (4 * Q)) & 15) + (l >> 1), rk = Q * 4 + (loc >> 2);
  const int cq = (xcd & 1) * 2 + (l & 1), ck = (xcd & 1) * 4 + (loc & 3);
  const int sq = 3 * (l >> 1) + 4 * (l & 1) + 1, sk = 2 * ((loc >> 2) + (loc & 3));
  int rbi = isq ? rq : rk, cg = isq ? cq : ck, c0 = isq ? sq : sk;
  c0 = c0 >= 9 ? c0 - 9 : c0;
  asm volatile("" : "+s"(rbi), "+s"(cg), "+s"(c0));
  PINS(x); PINS(w); PINS(wo); PINS(logA); PINS(Aim); PINS(Cre); PINS(Cim); PINS(logdt); PINS(Dsk); PINS(gw); PINS(ws);
  if (Q == 1 && l >= 4) return;
  if (__builtin_expect((loc < 16 || isq) && wave < 4, 1)) {
    role_load(L, x, w, wo, ws, isq, rbi, cg, c0, wave, lane STPASS);
  } else if (loc < 16) {
    role_kv(L, ws, rbi, cg, wave - 4, lane STPASS);
  } else if (isq) {
    role_q(L, ws, rbi, cg, wave - 4, lane STPASS);
  } else {
    role_ssm(L, x, logA, Aim, Cre, Cim, logdt, Dsk, ws, xcd * 9 + loc - 16, wave, lane STPASS);
  }
  STFLUSH(0);
}

constexpr int K2S = 27648, K2B = 5 * K2S, K2G = K2B + 10240;
template <int NR>
DI void k2_compute(lp L, char* out, int rt0, int r0, int cb, int ch, int lane STARG) {
  const int fr = lane & 15, fh = lane >> 4;
  const int col = 32 * ch + 2 * fr;
  lp B0 = L + fh * 1024 + ((col ^ (col >> 4)) << 4), B1 = L + fh * 1024 + (((col + 1) ^ (col >> 4)) << 4);
  lp A = L + 12288 + r0 * 3072 + lane * 16;
  lp B0h = B0 + 2 * K2S, B1h = B1 + 2 * K2S, Ah = A + 2 * K2S;
  lp Be = L + fh * 256 + col * 4, Ae = L + 12288 + r0 * 3072 + (lane & 31) * 16 + (lane >> 5) * 8;
  lp Bb = L + K2B + (32 * ch + fr) * 160 + ((fh >> 1) * 5 + r0) * 16 + (fh & 1) * 8, Gp = L + K2G + r0 * 64 + fh * 16;
  u32 dst = ((rt0 + r0) * 16 + fh * 4) * XROW + cb * 128 + col * 2;
  PIN(B0); PIN(B1); PIN(A); PIN(B0h); PIN(B1h); PIN(Ah); PIN(Be); PIN(Ae); PIN(Bb); PIN(Gp); PIN(dst); PINS(out);
  f32x4 acc[NR][2], cst[NR][2], g[NR];
  u32x4 a[2][NR], b[2][2];
  auto rd = [&](int n, int k) {
    const int sl = n / 3, s = n % 3, q = n & 1;
    if (k > NR + 1) return;
    const int t = k == 0 ? 0 : 1, i = k - 1;
    const bool isb = k == 0 || k == NR + 1;
    if (sl == 0 && s > 0) {
      if (isb) { lp e = Be + s * 4096 + t * 4; b[q][t] = u32x4{r32(e), r32(e + 1024), r32(e + 2048), r32(e + 3072)}; }
      else {
        const u32x2 lo = r64(Ae + i * 3072 + s * 1024), hi = r64(Ae + i * 3072 + s * 1024 + 512);
        a[q][i] = u32x4{lo[0], lo[1], hi[0], hi[1]};
      }
    } else {
      const bool up = sl >= 2 && sl < 5;
      const int o = (sl == 5 ? 0 : up ? sl - 2 : sl) * K2S;
      if (isb) b[q][t] = r128((t ? (up ? B1h : B1) : (up ? B0h : B0)) + o + s * 4096);
      else a[q][i] = r128((up ? Ah : A) + o + i * 3072 + s * 1024);
    }
    SCHED();
  };
  BAR();
  STAMP(1);
#pragma unroll
  for (int k = 0; k < NR + 2; ++k) rd(0, k);
#pragma unroll
  for (int n = 0; n < 18; ++n) {
    if (n % 3 == 2 && n < 17) { BAR(); STAMP(2 + n / 3); }
    if (n < 17) { rd(n + 1, 0); rd(n + 1, 1); }
#pragma unroll
    for (int m = 0; m < 2 * NR; ++m) {
      const int t = m / NR, i = m % NR;
      acc[i][t] = mfma(a[n & 1][i], b[n & 1][t], n ? acc[i][t] : f32x4{0.f, 0.f, 0.f, 0.f});
      SCHED();
      if (n < 17) rd(n + 1, m + 2);
    }
    if (n == CSTN) {
      u32x2 bv[NR][2];
#pragma unroll
      for (int i = 0; i < NR; ++i) {
        g[i] = __builtin_bit_cast(f32x4, r128(Gp + i * 64));
        bv[i][0] = r64(Bb + i * 16); SCHED(); bv[i][1] = r64(Bb + 2560 + i * 16); SCHED();
      }
      SCHED();
#pragma unroll
      for (int i = 0; i < NR; ++i)
#pragma unroll
        for (int t = 0; t < 2; ++t) {
          cst[i][t] = f32x4{bf2f(bv[i][t][0]), bfhi(bv[i][t][0]), bf2f(bv[i][t][1]), bfhi(bv[i][t][1])} * (1.f - g[i]);
          PIN(cst[i][t]);
        }
      SCHED();
    }
  }
  STAMP(7);
  const rsrc_t ro = __builtin_amdgcn_make_buffer_rsrc(out, 0, SEQ * XROW, 0x00020000);
#pragma unroll
  for (int i = 0; i < NR; ++i) {
    f32x4 u = acc[i][0] * g[i] + cst[i][0], v = acc[i][1] * g[i] + cst[i][1];
    PIN(u); PIN(v);
#pragma unroll
    for (int j = 0; j < 4; ++j) __builtin_amdgcn_raw_buffer_store_b32(pk(u[j], v[j]), ro, dst + j * XROW, 16 * i * XROW, AUX_OUT);
  }
}

extern "C" __global__ __launch_bounds__(512, 2) void gm_k2(const char* ws, char* out) {
  extern __shared__ __attribute__((aligned(16))) char smem[];
  lp L = (lp)smem;
  STDECL;
  STAMP(0);
  const int xcd = blockIdx.x & 7, id = xcd * 29 + (xcd < 2 ? xcd : 2) + (blockIdx.x >> 3);
  const int rb = (id * 7282) >> 16, cb = id - rb * 9;
  const int wave = __builtin_amdgcn_readfirstlane(threadIdx.x >> 6), lane = threadIdx.x & 63;
  if (wave < 4) {
    __builtin_amdgcn_s_setprio(2);
    const int t1 = min(5 * rb + wave, 127), t4 = min(5 * rb + 4, 127);
    const u32 vm = WS_M + cb * 73728 + wave * 3072 + lane * 16;
    const u32 v1 = WS_PQ + t1 * 18432 + lane * 16, v4 = WS_PQ + t4 * 18432 + wave * 768 + lane * 16;
    auto issue = [&](int sl) {
      const u32 gq = sl == 0 ? 5 : sl - 1;
      const u32 lb = (sl == 5 ? 0 : sl) * K2S;
      const char* pb = ws + gq * 3072;
      glds3(ws + gq * 12288, lb + wave * 3072, vm);
      glds3(pb, lb + 12288 + wave * 3072, v1);
      asm volatile("s_mov_b32 m0, %2\n\ts_mov_b32 exec_hi, 0xffff\n\tglobal_load_lds_dwordx4 %0, %1\n\ts_mov_b32 exec_hi, -1"
                   ::"v"(v4), "s"(pb), "s"(lb + 24576 + wave * 768) : "memory");
    };
    STAMP(8);
    issue(0); issue(1); issue(2);
    SCHED();
    u32 bo[3];
#pragma unroll
    for (int i = 0; i < 3; ++i) {
      const int f = i * 64 + (i < 2 ? lane : lane & 31);
      const int ir = (f * 6554) >> 16, pc = f - ir * 10, hh = pc >= 5;
      const int rt = min(5 * rb + pc - 5 * hh, 127);
      bo[i] = ir * (2 * BTP) + (rt >> 2) * 128 + hh * 64 + (rt & 3) * 16 - (i == 1 ? 1024 : 0);
      PIN(bo[i]);
    }
    const char* bt = ws + (WS_BT + (cb * 64 + (wave >> 1) * 32 + (wave & 1)) * BTP);
    STAMP(9);
    waitv<14>(); BAR();
    issue(3);
    waitv<14>(); BAR();
    if (wave == 3 && lane < 20) glds(ws + WS_G, min(rb * 320 + lane * 16, SEQ * 4 - 16), K2G);
    glds2(bt, K2B + wave * 2560, bo[0], bo[1]);
    glds_lo(bt, bo[2], K2B + wave * 2560 + 2048);
    issue(4); waitv<17>(); BAR();
    issue(5); waitv<17>(); BAR();
    waitv<7>(); BAR();
    waitv<0>(); BAR();
  } else {
    if (wave < 6) k2_compute<3>(L, out, 5 * rb, 0, cb, wave & 1, lane STPASS);
    else k2_compute<2>(L, out, 5 * rb, 3, cb, wave & 1, lane STPASS);
  }
  STFLUSH(1);
}

extern "C" __device__ __attribute__((used, noinline)) void gm_code_end() {}

extern "C" void kernel_launch(void* const* d_in, const int* in_sizes, int n_in, void* d_out, int out_size, void* d_ws,
                              size_t ws_size, hipStream_t stream) {
  (void)hipFuncSetAttribute((const void*)gm_k1, hipFuncAttributeMaxDynamicSharedMemorySize, K1_LDS);
  (void)hipFuncSetAttribute((const void*)gm_k2, hipFuncAttributeMaxDynamicSharedMemorySize, K2_LDS);
  gm_k1<<<256, 512, K1_LDS, stream>>>((const char*)d_in[0], (const char*)d_in[1], (const char*)d_in[2], (const u32*)d_in[4],
                                   (const u32*)d_in[5], (const u32*)d_in[6], (const u32*)d_in[7], (const u16*)d_in[8],
                                   (const u16*)d_in[9], (const char*)d_in[10], (char*)d_ws, (const char*)d_out);
  gm_k2<<<234, 512, K2_LDS, stream>>>((const char*)d_ws, (char*)d_out);
}
```

```cpp
#include <hip/hip_runtime.h>
#include <stdint.h>
#define PAD1 3000
#define PAD2 1000

typedef unsigned int u32;
typedef unsigned short u16;
typedef unsigned long long u64;
typedef __attribute__((ext_vector_type(2))) unsigned u32x2;
typedef __attribute__((ext_vector_type(4))) unsigned u32x4;
typedef __attribute__((ext_vector_type(2))) float f32x2;
typedef __attribute__((ext_vector_type(4))) float f32x4;
typedef __attribute__((ext_vector_type(4))) short s16x4;
typedef __attribute__((ext_vector_type(2))) __bf16 bf16x2;
typedef __attribute__((ext_vector_type(8))) __bf16 bf16x8;
#define LAS __attribute__((address_space(3)))
#define GAS __attribute__((address_space(1)))
#define DI __device__ __forceinline__
typedef LAS char* lp;

constexpr int SEQ = 2048, DM = 576;
constexpr int XROW = DM * 2;
constexpr int WROW = 3 * DM * 2;
constexpr int WS_PQ = 0;
constexpr int WS_M = WS_PQ + 128 * 18 * 1024;
constexpr int WS_BT = WS_M + 36 * 18 * 1024;
#define BTP 4224
constexpr int WS_G = WS_BT + DM * BTP;
constexpr int WS_MBKV = WS_G + SEQ * 4;
constexpr int MBKV_SZ = 16 * 640;
constexpr int WS_MBZ = WS_MBKV + 128 * MBKV_SZ;
constexpr int MBZ_SZ = 2 * 16 * 384;
constexpr int WS_EP = WS_MBZ + 44 * MBZ_SZ;
constexpr int WS_END = WS_EP + 176 * 128;
constexpr int K1_LDS = 162304;
constexpr int K2_LDS = 5 * 27648 + 10240 + 320 + 64;
#define PF_BLOCKS 1
#define CSTN 13
#define K2LA 3
#define ROT1 1
#define AUX_OUT 16

DI float bf2f(u32 v) { return __uint_as_float(v << 16); }
DI float bfhi(u32 v) { return __uint_as_float(v & 0xffff0000u); }
DI u32 pk(float a, float b) { bf16x2 v = {(__bf16)a, (__bf16)b}; return __builtin_bit_cast(u32, v); }
DI float rbf(float a) { return bf2f(pk(a, a) & 0xffffu); }
DI f32x4 mfma(u32x4 a, u32x4 b, f32x4 c) {
  return __builtin_amdgcn_mfma_f32_16x16x32_bf16(__builtin_bit_cast(bf16x8, a), __builtin_bit_cast(bf16x8, b), c, 0, 0, 0);
}
DI u32x4 r128(lp a) { return *(LAS u32x4*)a; }
DI u32x2 r64(lp a) { return *(LAS u32x2*)a; }
DI u32 r32(lp a) { return *(LAS u32*)a; }
DI void w128(lp a, u32x4 v) { *(LAS u32x4*)a = v; }
DI void w64(lp a, u32x2 v) { *(LAS u32x2*)a = v; }
DI void w32(lp a, u32 v) { *(LAS u32*)a = v; }
DI void w16(lp a, u32 v) { *(LAS u16*)a = (u16)v; }
DI u32x2 rtr(lp a) { return __builtin_bit_cast(u32x2, __builtin_amdgcn_ds_read_tr16_b64_v4i16((LAS s16x4*)a)); }
DI u32x4 rtr2(lp a0, lp a1) { u32x2 l = rtr(a0), h = rtr(a1); return u32x4{l[0], l[1], h[0], h[1]}; }
DI u32x4 r4x32(lp a) { return u32x4{r32(a), r32(a + 4), r32(a + 8), r32(a + 12)}; }
DI u32x4 r2x2(lp a, lp b) { return u32x4{r32(a), r32(a + 4), r32(b), r32(b + 4)}; }
DI void glds(const void* sbase, u32 voff, u32 lds) {
  asm volatile("s_mov_b32 m0, %2\n\ts_nop 0\n\tglobal_load_lds_dwordx4 %0, %1" ::"v"(voff), "s"(sbase), "s"(lds) : "memory");
}
DI void glds4(const void* sbase, u32 lds, u32 v0, u32 v1, u32 v2, u32 v3) {
  asm volatile("s_mov_b32 m0, %5\n\ts_nop 0\n\tglobal_load_lds_dwordx4 %0, %4\n\tglobal_load_lds_dwordx4 %1, %4 offset:1024\n\t"
               "global_load_lds_dwordx4 %2, %4 offset:2048\n\tglobal_load_lds_dwordx4 %3, %4 offset:3072"
               ::"v"(v0), "v"(v1), "v"(v2), "v"(v3), "s"(sbase), "s"(lds) : "memory");
}
DI void glds2(const void* sbase, u32 lds, u32 v0, u32 v1) {
  asm volatile("s_mov_b32 m0, %3\n\ts_nop 0\n\tglobal_load_lds_dwordx4 %0, %2\n\tglobal_load_lds_dwordx4 %1, %2 offset:1024"
               ::"v"(v0), "v"(v1), "s"(sbase), "s"(lds) : "memory");
}
DI void glds3(const void* sbase, u32 lds, u32 v) {
  asm volatile("s_mov_b32 m0, %2\n\ts_nop 0\n\tglobal_load_lds_dwordx4 %0, %1\n\tglobal_load_lds_dwordx4 %0, %1 offset:1024\n\t"
               "global_load_lds_dwordx4 %0, %1 offset:2048" ::"v"(v), "s"(sbase), "s"(lds) : "memory");
}
DI void glds_lo(const void* sbase, u32 voff, u32 lds) {
  asm volatile("s_mov_b32 m0, %2\n\ts_mov_b32 exec_hi, 0\n\tglobal_load_lds_dwordx4 %0, %1\n\ts_mov_b32 exec_hi, -1"
               ::"v"(voff), "s"(sbase), "s"(lds) : "memory");
}
DI void touch(const void* sbase, u32 voff) {
  asm volatile("s_mov_b32 m0, %2\n\ts_nop 0\n\tglobal_load_lds_dword %0, %1 sc1" ::"v"(voff), "s"(sbase), "s"(K1_LDS - 256) : "memory");
}
template <int N> DI void waitv() { asm volatile("s_waitcnt vmcnt(%0)" ::"i"(N) : "memory"); }
#define CFENCE() asm volatile("" ::: "memory")
#define WAITL0() do { __builtin_amdgcn_s_waitcnt(0xC07F); CFENCE(); } while (0)
#define BAR() do { CFENCE(); __builtin_amdgcn_s_barrier(); CFENCE(); } while (0)
#define SCHED() __builtin_amdgcn_sched_barrier(0)
#define PIN(x) asm volatile("" : "+v"(x))
#define PINS(x) asm volatile("" ::"s"(x))
constexpr float EPS = 1e-6f, L2E = 1.44269504089f;
DI f32x2 phi2(float a0, float a1) {
  f32x2 m = f32x2{a0, a1} * L2E - EPS * L2E;
  f32x2 e = {__builtin_amdgcn_fmed3f(__builtin_amdgcn_exp2f(m[0]), 0.f, 1.f), __builtin_amdgcn_fmed3f(__builtin_amdgcn_exp2f(m[1]), 0.f, 1.f)};
  const int ei = __builtin_bit_cast(int, EPS);
  f32x2 x = {__int_as_float(max(__float_as_int(a0), ei)), __int_as_float(max(__float_as_int(a1), ei))};
  return x + e;
}
DI f32x4 phi4(f32x4 a) { f32x2 l = phi2(a[0], a[1]), h = phi2(a[2], a[3]); return f32x4{l[0], l[1], h[0], h[1]}; }
DI float rowsum16(float v) {
  v += __builtin_bit_cast(float, __builtin_amdgcn_update_dpp(0, __builtin_bit_cast(int, v), 0x128, 0xf, 0xf, false));
  v += __builtin_bit_cast(float, __builtin_amdgcn_update_dpp(0, __builtin_bit_cast(int, v), 0x124, 0xf, 0xf, false));
  v += __builtin_bit_cast(float, __builtin_amdgcn_update_dpp(0, __builtin_bit_cast(int, v), 0x122, 0xf, 0xf, false));
  v += __builtin_bit_cast(float, __builtin_amdgcn_update_dpp(0, __builtin_bit_cast(int, v), 0x121, 0xf, 0xf, false));
  return v;
}
DI float prodsum(float a, float b, float c, float d) {
  auto s0 = __builtin_amdgcn_permlane32_swap(__float_as_uint(a), __float_as_uint(c), false, false);
  auto s1 = __builtin_amdgcn_permlane32_swap(__float_as_uint(b), __float_as_uint(d), false, false);
  const float ac = __uint_as_float(s0[0]) + __uint_as_float(s0[1]), bd = __uint_as_float(s1[0]) + __uint_as_float(s1[1]);
  auto s2 = __builtin_amdgcn_permlane16_swap(__float_as_uint(ac), __float_as_uint(bd), false, false);
  float v = __uint_as_float(s2[0]) + __uint_as_float(s2[1]);
  v += __builtin_bit_cast(float, __builtin_amdgcn_update_dpp(0, __builtin_bit_cast(int, v), 0x128, 0xf, 0xf, false));
  v += __builtin_bit_cast(float, __builtin_amdgcn_update_dpp(0, __builtin_bit_cast(int, v), 0x124, 0xf, 0xf, false));
  return v;
}
DI float prodsum(u32x4 m) { return prodsum(bf2f(m[0]), bfhi(m[0]), bf2f(m[2]), bfhi(m[2])); }
DI u32x4 pack2(f32x4 a, f32x4 b) { return u32x4{pk(a[0], a[1]), pk(a[2], a[3]), pk(b[0], b[1]), pk(b[2], b[3])}; }
typedef __amdgpu_buffer_rsrc_t rsrc_t;
DI rsrc_t wsrsrc(char* ws) { return __builtin_amdgcn_make_buffer_rsrc(ws, 0, WS_END, 0x00020000); }
DI void st_sc1(rsrc_t r, u32 off, u32x4 v) { __builtin_amdgcn_raw_buffer_store_b128(v, r, off, 0, 16); }
DI u32x4 ld_sc1(rsrc_t r, u32 off) { return __builtin_amdgcn_raw_buffer_load_b128(r, off, 0, 16); }
DI void st32_sc1(rsrc_t r, u32 off, u32 v) { __builtin_amdgcn_raw_buffer_store_b32(v, r, off, 0, 16); }
DI u32 ld_ep(char* ws, int i) { return __hip_atomic_load((GAS u32*)(ws + WS_EP) + 32 * i, __ATOMIC_RELAXED, __HIP_MEMORY_SCOPE_AGENT); }
DI void st_ep(rsrc_t r, int i, int lane, u32 v) { if (lane < 8) st_sc1(r, WS_EP + 128 * i + 16 * lane, u32x4{v, v, v, v}); }
constexpr u32 OOR = 0x7ffffff0u;
constexpr u32 SPIN_CAP = 1u << 15;
#define SENT_SLEEP 4
#define POLL_SLEEP 4
#define STDECL
#define STARG
#define STPASS
#define STAMP(i)
#define STFLUSH(k)

constexpr int XR_KV = 4 * 18432, XR_Q = 3 * 18432;
constexpr int GWL = XR_KV + 4 * 16384;
constexpr int WOI = GWL + 1280;
constexpr int XCH = XR_KV + 16384, KVA = 18432;

template <int ROLE>
DI void gemm_main(lp L, int rg, int lane, f32x4 (&acc)[ROLE ? 3 : 2][9] STARG) {
  constexpr int RT = ROLE ? 3 : 2, XB = RT * 8192, XRING = ROLE ? XR_Q : XR_KV, NS = ROLE ? 3 : 4;
  const int fr = lane & 15, fh = lane >> 4;
  const int q4 = (lane >> 2) & 3, p4 = lane & 3;
  const int ax0 = XRING + (rg * RT * 16 + fr) * 128 + ((fh ^ ((fr >> 1) & 7)) << 4), ax1 = ax0 ^ 64;
  const int r0 = (8 * fh + q4 + 4 * (fh & 1)) * 288, r1 = (8 * fh + q4 + 4 * ((fh & 1) ^ 1)) * 288;
  const int cm = ROLE ? 72 * p4 : 8 * p4, cx = 72 * p4 + 32;
  u32x4 a0[RT], a1[RT], b0[9], b1[9];
  lp W0 = L + r0 + cm, W1 = L + r1 + cm, E0 = L + r0 + cx, E1 = L + r1 + cx, X0 = L + ax0, X1 = L + ax1;
  PIN(E0); PIN(E1);
  auto step = [&](bool domma, u32x4 (&a)[RT], u32x4 (&b)[9], int slot, int s, u32x4 (&an)[RT], u32x4 (&bn)[9]) {
    auto rd = [&](int n) {
      if (n < RT) { an[n] = r128((s ? X1 : X0) + slot * XB + n * 2048); return; }
      n -= RT;
      if (n >= 18) return;
      const int t = n >> 1, so = slot * 18432 + s * 9216;
      const int to = (ROLE ? 8 * t : 72 * (t >> 1) + 40 * (t & 1)) + so;
      const u32x2 v = (ROLE || t < 8) ? rtr((n & 1 ? W1 : W0) + to) : rtr((n & 1 ? E1 : E0) + so);
      bn[t][2 * (n & 1)] = v[0]; bn[t][2 * (n & 1) + 1] = v[1];
    };
    const int EX = ROLE ? 0 : (s ? 7 : 2);
#pragma unroll
    for (int m = 0; m < 9 * RT; ++m) {
      const int t = m / RT, i = m % RT;
      if (domma) acc[i][t] = ROLE ? mfma(b[t], a[i], acc[i][t]) : mfma(a[i], b[t], acc[i][t]);
      SCHED();
      if (m < 2 * EX) { rd(m + (m + 1) / 2); if (!(m & 1)) rd(m + m / 2 + 1); } else rd(m + EX);
      SCHED();
    }
  };
#pragma unroll
  for (int i = 0; i < RT; ++i)
#pragma unroll
    for (int t = 0; t < 9; ++t) { const float v = (ROLE || t < 4 || (t == 8 && fr < 8)) ? EPS : 0.f; acc[i][t] = f32x4{v, v, v, v}; PIN(acc[i][t]); }
  STAMP(1);
  BAR();
  STAMP(2);
  step(false, a1, b1, 0, 0, a0, b0);
#pragma unroll 1
  for (int it = 0; it < (ROLE ? 3 : 2); ++it) {
#pragma unroll
    for (int sl = 0; sl < NS; ++sl) {
      step(true, a0, b0, sl, 1, a1, b1);
      WAITL0();
      BAR();
      SCHED();
      step(true, a1, b1, sl == NS - 1 ? 0 : sl + 1, 0, a0, b0);
    }
  }
  if (ROLE == 0) {
    step(true, a0, b0, 0, 1, a1, b1);
#pragma unroll
    for (int t = 0; t < 9; ++t)
#pragma unroll
      for (int i = 0; i < RT; ++i) acc[i][t] = mfma(a1[i], b1[t], acc[i][t]);
  }
}

DI void kv_consume(lp L, rsrc_t rs, int rb, int g, u32 tme, int wave, int lane STARG) {
  const int chead = 4 * g + (rb & 3), crole = rb >> 2;
  if (crole == 3) return;
  const int fr = lane & 15, fh = lane >> 4;
  const bool ext = crole == 2;
  u32x4 wf[6];
  {
    const int codd = chead & 1, q4 = (lane >> 2) & 3, p4 = lane & 3;
    int s0 = 8 * fh + q4, s1 = s0 + 4;
    s0 = s0 > 17 ? 17 : s0; s1 = s1 > 17 ? 17 : s1;
    s0 += 2 * codd; s1 += 2 * codd;
    s0 = s0 >= 18 ? s0 - 18 : s0; s1 = s1 >= 18 ? s1 - 18 : s1;
    lp p0 = L + WOI + s0 * XROW + wave * 64 + p4 * 8, p1 = L + WOI + s1 * XROW + wave * 64 + p4 * 8;
#pragma unroll
    for (int i = 0; i < 6; ++i) wf[i] = rtr2(p0 + (i >> 1) * 512 + (i & 1) * 32, p1 + (i >> 1) * 512 + (i & 1) * 32);
  }
  const int un = 2 * (chead >> 3) + (chead & 1), hh = (chead >> 1) & 3;
  const int col = (wave & 1) * 32 + (lane & 31);
  u32 mo = WS_M + (wave >> 1) * 73728 + (ext ? 65536 + (un * 4 + hh) * 256 + col * 4 : (2 * un + crole) * 4096 + hh * 1024 + ((col ^ (col >> 4)) << 4));
  if (lane >= 32) mo = OOR;
  lp afp = L + KVA + fr * 64 + fh * 16;
  PIN(mo); PIN(afp);
  waitv<0>();
  STAMP(6);
  if (wave < (ext ? 5 : 8)) {
    const int it = 4 * wave + (lane & 3);
    const u32 off = WS_MBKV + (g * 16 + rb) * MBKV_SZ + (lane >> 2) * 640 + it * 16;
    const bool two = wave == 0 && !ext;
    const bool keep = fr < 4 && (!ext || (it < 18 && (fh >> 1) == (chead & 1)));
    lp d = L + KVA + (!keep ? 512 + lane * 2 : ext ? (fh & 1) * 64 + it * 2 : ((it >> 4) * 4 + fh) * 64 + (it & 15) * 2);
    lp d2 = L + KVA + (fr < 4 ? ((fr >> 1) * 4 + fh) * 64 + 32 + (fr & 1) * 2 : 512 + lane * 2);
    PIN(d); PIN(d2);
    u32x4 m, e = {0, tme, 0, tme};
    for (u32 spin = 0;; ++spin) {
      m = ld_sc1(rs, off);
      if (two) e = ld_sc1(rs, off + 512);
      bool ok = m[1] == tme && m[3] == tme && e[1] == tme && e[3] == tme;
      if (__all(ok) || spin > SPIN_CAP) break;
      __builtin_amdgcn_s_sleep(POLL_SLEEP);
    }
    STAMP(7);
    const float sm = prodsum(m);
    w16(d, pk(sm, sm));
    if (two) { const float se = prodsum(e); w16(d2, pk(se, se)); }
  }
  WAITL0();
  BAR();
  STAMP(8);
  const u32x4 af = r128(afp);
#pragma unroll
  for (int i = 0; i < 3; ++i) {
    if (i == 2 && wave >= 2) break;
    const f32x4 u = mfma(af, wf[2 * i], f32x4{0.f, 0.f, 0.f, 0.f}), v = mfma(af, wf[2 * i + 1], f32x4{0.f, 0.f, 0.f, 0.f});
    auto sx = __builtin_amdgcn_permlane16_swap(pk(u[0], u[1]), pk(v[0], v[1]), false, false);
    auto sy = __builtin_amdgcn_permlane16_swap(pk(u[2], u[3]), pk(v[2], v[3]), false, false);
    if (ext) st32_sc1(rs, mo + i * (4 * 73728), sx[0]);
    else st_sc1(rs, mo + i * (4 * 73728), u32x4{sx[0], sy[0], sx[1], sy[1]});
  }
  if (wave == 7) st_ep(rs, g * 16 + rb, lane, tme);
}

DI void role_load(lp L, const char* x, const char* w, const char* wo_g, char* ws, bool isq, int rbi, int cg, int c0,
                  int lw, int lane STARG) {
  __builtin_amdgcn_s_setprio(2);
  const bool six = isq && rbi < 10;
  const int XP = isq ? 6 : 4;
  const char* xr = x + rbi * (isq ? 192 * XROW : 128 * XROW);
  const u32 xl = (isq ? XR_Q : XR_KV) + lw * XP * 1024, XB = XP * 4096;
  u32 xo[4], wo[5];
  xo[0] = (8 * (six || !isq ? lw * XP : (lw * XP > 12 ? 12 : lw * XP)) + (lane >> 3)) * XROW + (((lane & 7) ^ (lane >> 4)) << 4);
  xo[1] = (xo[0] ^ 64) + 8192; xo[2] = xo[0] + 16384; xo[3] = xo[1] + 16384;
  auto issx = [&](int cidx, int slot) {
    const char* xb = xr + cidx * 128;
    glds4(xb, xl + slot * XB, xo[0], xo[1], xo[2], xo[3]);
    if (isq) glds2(xb + (six || lw < 2 ? 32 * XROW : 0), xl + slot * XB + 4096, xo[0], xo[1]);
  };
  auto issw = [&](int cidx, int slot) {
    const char* wb = w + cidx * (64 * WROW);
    const u32 lb = slot * 18432 + lw * 4608;
    glds4(wb, lb, wo[0], wo[1], wo[2], wo[3]);
    glds_lo(wb, wo[4], lb + 4096);
  };
  auto nxt = [](int c) { return c == 8 ? 0 : c + 1; };
  STAMP(3);
  issx(c0, 0);
  SCHED();
  {
    const int cb0 = (isq ? 288 * cg : 1152 + 144 * cg) + 16 * lw * WROW, cb1 = isq ? cb0 : cb0 + 1008;
#pragma unroll
    for (int i = 0; i < 5; ++i) {
      const int f = i < 4 ? 64 * i + lane : 256 + (lane & 31);
      const int lr = (f * 3641) >> 16, cc = f - lr * 18;
      wo[i] = (lr ^ ((lr >> 1) & 4)) * WROW + (cc < 9 ? cb0 : cb1) + 16 * cc - (i < 4 ? 1024 * i : 0);
    }
  }
  issw(c0, 0);
  STAMP(9);
  int c = nxt(c0);
  const int fr = lane & 15, fh = lane >> 4;
  const int me = isq ? 128 + cg * 11 + rbi : cg * 16 + rbi;
  const bool isz = lane < 48;
  const int jq = lane >> 2, cz = 128 + (cg >> 1) * 11 + (jq > 10 ? 10 : jq);
  const int ca = isz ? cz : cg * 16 + lw + ((lane >> 1) & 4), cb_ = isz ? cz : cg * 16 + lw + 8;
  u32 tme, ta, tb;
  if (isq) {
    issx(c, 1); issw(c, 1);
    c = nxt(c);
    int sx = 2;
    STAMP(1);
    waitv<11>();
    BAR();
    STAMP(2);
#pragma unroll 1
    for (int k = 0; k < 7; ++k) {
      issx(c, sx); issw(c, sx);
      c = nxt(c);
      sx = sx == 2 ? 0 : sx + 1;
      waitv<11>();
      BAR();
    }
    waitv<0>();
    tme = ld_ep(ws, me); ta = tb = 0;
    BAR();
  } else {
    issx(c, 1); issw(c, 1);
    c = nxt(c);
    const char* wb = w + c * (64 * WROW);
    const char* xb = xr + c * 128;
    asm volatile("" : "+s"(wb), "+s"(xb));
    STAMP(1);
    waitv<9>();
    BAR();
    STAMP(2);
    glds4(xb, xl + 2 * 16384, xo[0], xo[1], xo[2], xo[3]);
#pragma unroll
    for (int k = 0; k < 7; ++k) {
      const u32 lb = ((k + 2) & 3) * 18432 + lw * 4608;
      glds4(wb, lb, wo[0], wo[1], wo[2], wo[3]);
      glds_lo(wb, wo[4], lb + 4096);
      if (k < 6) {
        c = nxt(c);
        xb = xr + c * 128; wb = w + c * (64 * WROW);
        asm volatile("" : "+s"(wb), "+s"(xb));
        glds4(xb, xl + ((k + 3) & 3) * 16384, xo[0], xo[1], xo[2], xo[3]);
        waitv<13>();
      } else {
        waitv<9>();
      }
      BAR();
    }
    if (rbi < 12) {
      const char* src = wo_g + (4 * cg + (rbi & 3)) * (18 * XROW) + lw * 5184;
      glds4(src, WOI + lw * 5184, lane * 16, lane * 16, lane * 16, lane * 16);
      glds(src + 4096, lane * 16, WOI + lw * 5184 + 4096);
      if (lane < 4) glds(src + 5120, lane * 16, WOI + lw * 5184 + 5120);
      waitv<6>();
    } else {
      waitv<0>();
    }
    BAR();
    tme = ld_ep(ws, me); ta = ld_ep(ws, ca); tb = ld_ep(ws, cb_);
  }
  const rsrc_t rs = wsrsrc(ws);
  tme += 1; ta += 1; tb += 1;
  if (isq) {
    BAR();
    const int i0 = lw * 4 + (lane & 3);
    const u32 base = WS_MBZ + (me - 128) * MBZ_SZ + (lane >> 2) * 384;
    u32 o[3]; lp zw[3];
#pragma unroll
    for (int i = 0; i < 3; ++i) {
      const int it = i0 + 16 * i, gi = it >= 20, r = it - 20 * gi;
      o[i] = base + gi * (MBZ_SZ / 2) + r * 16;
      zw[i] = L + GWL + (r < 16 ? (2 * gi + (r >> 3)) * 160 + (r & 7) * 16 : (2 * gi + (r & 1)) * 160 + 128 + (r & 2) * 8) + fh * 4;
    }
    const u32 so = __builtin_amdgcn_readfirstlane(base) + (lw & 1) * (MBZ_SZ / 2) + ((4 * lw + rbi) & 15) * 384 + 324;
    for (u32 spin = 0; spin < SPIN_CAP; ++spin) {
      if (__builtin_amdgcn_readfirstlane(__builtin_amdgcn_raw_buffer_load_b32(rs, 0, so, 16)) == __builtin_amdgcn_readfirstlane(tme)) break;
      __builtin_amdgcn_s_sleep(SENT_SLEEP);
    }
    STAMP(6);
    u32x4 m[3];
    m[0] = m[1] = u32x4{0, tme + 1, 0, tme};
    m[2] = u32x4{0, lw < 2 ? tme + 1 : tme, 0, tme};
    for (u32 spin = 0;; ++spin) {
      bool ok = true;
#pragma unroll
      for (int i = 0; i < 3; ++i) { const u32x4 v = ld_sc1(rs, m[i][1] == tme && m[i][3] == tme ? OOR : o[i]); if (!(m[i][1] == tme && m[i][3] == tme)) m[i] = v; }
#pragma unroll
      for (int i = 0; i < 3; ++i) ok = ok && m[i][1] == tme && m[i][3] == tme;
      if (__all(ok) || spin > SPIN_CAP) break;
      __builtin_amdgcn_s_sleep(POLL_SLEEP);
    }
    STAMP(7);
#pragma unroll
    for (int i = 0; i < 3; ++i) {
      if (i == 2 && lw >= 2) break;
      const float sv = prodsum(m[i]);
      if (fr < 4) w32(zw[i], __float_as_uint(sv));
    }
    WAITL0();
    BAR();
    if (lw == 0) st_ep(rs, me, lane, tme);
    return;
  }
  __builtin_amdgcn_s_setprio(0);
  if (lw == 0) w128(L + KVA + lane * 16, u32x4{0, 0, 0, 0});
  const u32 zb = WS_MBZ + (cz - 128) * MBZ_SZ + (cg & 1) * (MBZ_SZ / 2) + rbi * 384 + (lane & 3) * 16;
  u32 oa = isz ? (jq < 11 ? zb + lw * 64 : OOR) : WS_MBKV + ca * MBKV_SZ + rbi * 640 + 512 + (lane & 7) * 16;
  u32 ob = isz ? (jq < 11 && lw < 2 ? zb + 256 + lw * 64 : OOR) : lane < 56 ? WS_MBKV + cb_ * MBKV_SZ + rbi * 640 + 256 + (lane & 7) * 16 : OOR;
  int sa = isz ? (lane & 3) * 64 : ((((lane >> 2) & 2) + ((lane >> 1) & 1)) * 16 + 8 + 2 * lw + (lane & 1)) * 4;
  int sb = isz ? (lane & 3) * 64 : ((lw >> 1) * 16 + 8 + 2 * lw + (lane & 1)) * 4;
  lp bk = L + XCH + lane * 16 + lw * 1024, bx = L + XCH + lane * 16 + 8192;
  const bool own = (fr >> 1) == 4 + lw;
  PIN(oa); PIN(ob); PIN(sa); PIN(sb); PIN(bk); PIN(bx); PIN(ta); PIN(tb); PIN(tme);
  waitv<0>();
  WAITL0();
  BAR();
  STAMP(4);
  {
    u32x4 kf[4], xf[4];
#pragma unroll
    for (int r = 0; r < 4; ++r) { xf[r] = r128(bx + r * 9216); kf[r] = r128(bk + r * 9216); }
    SCHED();
    f32x4 p2 = {0.f, 0.f, 0.f, 0.f}, p4 = p2;
#pragma unroll
    for (int r = 0; r < 4; ++r) {
      u32x4 xo;
#pragma unroll
      for (int d = 0; d < 4; ++d) xo[d] = own ? xf[r][d] : 0x3f803f80u;
      p2 = mfma(kf[r], xo, p2); p4 = mfma(xf[r], xo, p4);
    }
    const u32 a0 = __builtin_amdgcn_ds_bpermute(sa, pk(p2[0], p2[1])), a1 = __builtin_amdgcn_ds_bpermute(sa, pk(p2[2], p2[3]));
    const u32 b0 = __builtin_amdgcn_ds_bpermute(sb, pk(p4[0], p4[1])), b1 = __builtin_amdgcn_ds_bpermute(sb, pk(p4[2], p4[3]));
    st_sc1(rs, oa, u32x4{a0, ta, a1, ta});
    st_sc1(rs, ob, u32x4{b0, tb, b1, tb});
  }
  STAMP(5);
  kv_consume(L, rs, rbi, cg, tme, lw, lane STPASS);
}

DI void role_kv(lp L, char* ws, int rb, int g, int hi, int lane STARG) {
  const int fr = lane & 15, fh = lane >> 4;
  const rsrc_t rs = wsrsrc(ws);
  const int c0 = g * 16 + hi + (lane < 32 ? 0 : 4), c1 = g * 16 + hi + 8;
  u32 d0 = WS_MBKV + c0 * MBKV_SZ + rb * 640 + (lane & 31) * 16;
  u32 d1 = fh == (hi >> 1) ? WS_MBKV + c1 * MBKV_SZ + rb * 640 + fr * 16 : OOR;
  u32 e0 = WS_EP + c0 * 128;
  lp xw = L + XCH + hi * 9216 + lane * 16, bk = L + XCH + hi * 1024 + lane * 16, bx = L + XCH + 8192 + lane * 16;
  PIN(d0); PIN(d1); PIN(e0); PIN(xw); PIN(bk); PIN(bx);
  f32x4 acc[2][9];
  gemm_main<0>(L, hi, lane, acc STPASS);
  u32 t0 = __builtin_amdgcn_raw_buffer_load_b32(rs, e0, 0, 16), t1 = __builtin_amdgcn_raw_buffer_load_b32(rs, 0, WS_EP + c1 * 128, 16);
  u32 tme = __builtin_amdgcn_raw_buffer_load_b32(rs, 0, WS_EP + (g * 16 + rb) * 128, 16);
  STAMP(3);
  {
#pragma unroll
    for (int t = 0; t < 4; ++t) {
      w128(xw + t * 1024, pack2(phi4(acc[0][t]), phi4(acc[1][t])));
      w128(xw + 4096 + t * 1024, pack2(acc[0][4 + t], acc[1][4 + t]));
    }
    const u32x4 xp = pack2(phi4(acc[0][8]), phi4(acc[1][8])), xr = pack2(acc[0][8], acc[1][8]);
    w128(xw + 8192, fr < 8 ? xp : xr);
  }
  t0 += 1; t1 += 1; tme += 1;
  PIN(t0); PIN(t1); PIN(tme);
  WAITL0();
  BAR();
  STAMP(4);
  {
    u32x4 xf[4], vf[4], kf[4];
#pragma unroll
    for (int r = 0; r < 4; ++r) { kf[r] = r128(bk + r * 9216); vf[r] = r128(bk + 4096 + r * 9216); }
    SCHED();
#pragma unroll
    for (int r = 0; r < 4; ++r) xf[r] = r128(bx + r * 9216);
    SCHED();
    f32x4 p1 = {0.f, 0.f, 0.f, 0.f}, p3 = p1;
#pragma unroll
    for (int r = 0; r < 4; ++r) p1 = mfma(kf[r], vf[r], p1);
#pragma unroll
    for (int r = 0; r < 4; ++r) p3 = mfma(xf[r], vf[r], p3);
    st_sc1(rs, d0, u32x4{pk(p1[0], p1[1]), t0, pk(p1[2], p1[3]), t0});
    st_sc1(rs, d1, u32x4{pk(p3[0], p3[1]), t1, pk(p3[2], p3[3]), t1});
  }
  STAMP(5);
  kv_consume(L, rs, rb, g, tme, 4 + hi, lane STPASS);
}

DI void role_q(lp L, char* ws, int jq, int hg, int rg, int lane STARG) {
  const int fh = lane >> 4;
  const rsrc_t rs = wsrsrc(ws);
  f32x4 acc[3][9];
  gemm_main<1>(L, rg, lane, acc STPASS);
  STAMP(3);
#pragma unroll
  for (int i = 0; i < 3; ++i)
#pragma unroll
    for (int t = 0; t < 9; ++t) { acc[i][t] = phi4(acc[i][t]); PIN(acc[i][t]); }
  const int rt0 = jq * 12 + rg * 3;
  u32 po = WS_PQ + rt0 * (18 * 1024) + hg * 4096 + lane * 16;
  u32 pe = WS_PQ + (rt0 + (lane >> 5)) * (18 * 1024) + 16384 + hg * 512 + (lane & 31) * 16;
  PIN(po); PIN(pe);
  STAMP(7);
  BAR();
  STAMP(8);
  f32x4 z[9];
#pragma unroll
  for (int t = 0; t < 9; ++t) z[t] = __builtin_bit_cast(f32x4, r128(L + GWL + fh * 160 + t * 16));
  u32x2 ex[3];
#pragma unroll
  for (int i = 0; i < 3; ++i) {
    const f32x4 x4 = acc[i][4] * z[8];
    f32x4 d4 = acc[i][0] * z[0] + acc[i][1] * z[1] + acc[i][2] * z[2] + acc[i][3] * z[3];
    f32x4 e4 = acc[i][5] * z[4] + acc[i][6] * z[5] + acc[i][7] * z[6] + acc[i][8] * z[7];
    f32x2 d2 = f32x2{d4[0], d4[1]} + f32x2{d4[2], d4[3]} + f32x2{x4[0], x4[1]};
    f32x2 e2 = f32x2{e4[0], e4[1]} + f32x2{e4[2], e4[3]} + f32x2{x4[2], x4[3]};
    const float r = __builtin_amdgcn_rcpf(d2[0] + d2[1] + EPS), s = __builtin_amdgcn_rcpf(e2[0] + e2[1] + EPS);
    const u32 p = rt0 + i < 128 ? po + i * (18 * 1024) : OOR;
    st_sc1(rs, p, pack2(acc[i][0] * r, acc[i][1] * r));
    st_sc1(rs, p + 1024, pack2(acc[i][2] * r, acc[i][3] * r));
    st_sc1(rs, p + 2048, pack2(acc[i][5] * s, acc[i][6] * s));
    st_sc1(rs, p + 3072, pack2(acc[i][7] * s, acc[i][8] * s));
    ex[i] = u32x2{pk(acc[i][4][0] * r, acc[i][4][1] * r), pk(acc[i][4][2] * s, acc[i][4][3] * s)};
    SCHED();
  }
#pragma unroll
  for (int k = 0; k < 2; ++k) {
    const int ib = k ? 2 : 1;
    auto sx = __builtin_amdgcn_permlane32_swap(ex[2 * k][0], ex[ib][0], false, false);
    auto sy = __builtin_amdgcn_permlane32_swap(ex[2 * k][1], ex[ib][1], false, false);
    const bool ok = rt0 + 2 * k + (lane >> 5) < 128 && (k == 0 || lane < 32);
    st_sc1(rs, ok ? pe + k * (36 * 1024) : OOR, u32x4{sx[0], sy[0], sx[1], sy[1]});
  }
}

struct cx2 { f32x2 r, i; };
DI cx2 cmul(cx2 a, cx2 b) { return cx2{a.r * b.r - a.i * b.i, a.r * b.i + a.i * b.r}; }
DI cx2 csq(cx2 a) { return cx2{a.r * a.r - a.i * a.i, (a.r + a.r) * a.i}; }
DI cx2 cpow(f32x2 lr, f32x2 rv, float n) {
  cx2 o;
#pragma unroll
  for (int m = 0; m < 2; ++m) {
    float mag = __builtin_amdgcn_exp2f(lr[m] * n), ph = __builtin_amdgcn_fractf(rv[m] * n);
    o.r[m] = mag * __builtin_amdgcn_cosf(ph);
    o.i[m] = mag * __builtin_amdgcn_sinf(ph);
  }
  return o;
}
constexpr int UB = 4608, PIT = 544, STG = UB, TBL = STG + 16 * PIT, WVB = TBL + 4368;
constexpr int TPE = TBL, TPD = 1216, TPO = TBL + TPD, F2 = TBL + 2368, F2S = 144, F3 = F2 + 4 * F2S, F3S = 176, FCP = 960;
static_assert(8 * WVB <= K1_LDS - 256 && WVB % 16 == 0, "lds");
struct SsmTab { u32 v[8][64][4]; };
constexpr SsmTab make_tab() {
  SsmTab t{};
  for (int l = 0; l < 64; ++l) {
    const int fr = l & 15, fh = l >> 4;
    const int lo[5] = {3, 6, 5, 4, 3}, hi[5] = {5, 10, 11, 12, 4};
    for (int k = 0; k < 5; ++k)
      for (int jj = 0; jj < 8; ++jj) {
        const int xx = fr + 16 - 8 * fh - jj;
        if (xx >= lo[k] && xx <= hi[k]) t.v[k][l][jj >> 1] |= (k == 4 ? 0xffffu : 0x3f80u) << (16 * (jj & 1));
      }
    for (int j = 0; j < 4; ++j)
      t.v[5][l][j] = fh < 2 ? F2 + (2 * (fr & 1) + (j & 1)) * F2S + (47 - 16 * fh - 8 * (j >> 1) - (fr >> 1)) * 2
                            : F3 + (fr & 1) * F3S + (63 - 32 * (fh - 2) - 8 * j - (fr >> 1)) * 2;
    t.v[6][l][0] = TBL + 1080 - 2 * (128 * (fh >> 1) + 4 * (fh & 1) + 8 * fr);
    t.v[6][l][1] = l < 32 ? TBL + 558 - 16 * l : STG + 14 + 16 * l;
    t.v[6][l][2] = l == 31 ? TBL + 60 : l == 63 ? TBL + 574 : l == 32 ? TBL + 1088 : STG + 4 * l;
    t.v[6][l][3] = l < 32 ? l * 16 : l < 36 ? TPE + (l - 32) * 16 : l < 40 ? TPE + 1088 + (l - 36) * 16
                   : l < 44 ? TPO + (l - 40) * 16 : l < 48 ? TPO + 1088 + (l - 44) * 16 : F2;
    t.v[7][l][0] = fr * 32 + fh * 16;
    t.v[7][l][1] = ((fr & 1) ? TPO + 2 : TPE) + (16 - fr + 8 * fh) * 2;
    t.v[7][l][2] = F2 + ((fr & 1) ? 0 : FCP + 2) + (15 - fr + 4 * fh) * 2;
    t.v[7][l][3] = STG + fr * PIT + fh * 16;
  }
  return t;
}
__device__ const SsmTab ssm_tab = make_tab();

DI void role_ssm(lp L, const char* x, const u32* logA, const u32* Aim, const u32* Cre, const u32* Cim, const u16* logdt,
                 const u16* Dsk, char* ws, int cb, int wave, int lane STARG) {
  const int chn = cb * 8 + wave;
  const GAS u32x4* tab = (const GAS u32x4*)ssm_tab.v[2] + lane;
  PIN(tab);
  u32 pa, pi, pcr, pci, pdt, pds;
  u32x4 o5, o6, o7;
  asm volatile("global_load_dword %0, %9, %12\n\tglobal_load_dword %1, %9, %13\n\tglobal_load_ushort %4, %10, %16\n\t"
               "global_load_dword %2, %9, %14\n\tglobal_load_dword %3, %9, %15\n\tglobal_load_ushort %5, %10, %17\n\t"
               "global_load_dwordx4 %6, %11, %18\n\tglobal_load_dwordx4 %7, %11, %18 offset:1024\n\tglobal_load_dwordx4 %8, %11, %18 offset:2048"
               : "=&v"(pa), "=&v"(pi), "=&v"(pcr), "=&v"(pci), "=&v"(pdt), "=&v"(pds), "=&v"(o5), "=&v"(o6), "=&v"(o7)
               : "v"((chn * 64 + lane) * 4), "v"(chn * 2), "v"(lane * 16), "s"(logA), "s"(Aim), "s"(Cre), "s"(Cim), "s"(logdt), "s"(Dsk),
                 "s"(ssm_tab.v[5])
               : "memory");
  lp wv = L + wave * WVB;
  const int tid = wave * 64 + lane;
  w128(wv + F2 + lane * 16, u32x4{0, 0, 0, 0});
  if (lane < 56) w128(wv + F2 + 1024 + lane * 16, u32x4{0, 0, 0, 0});
  u32x4 ur[4];
  const char* xc = x + cb * 16;
  const u32 uo = 4 * tid * XROW;
  auto ldu = [&](int i) { SCHED(); ur[i] = *(const u32x4*)(xc + uo + i * XROW); SCHED(); };
  BAR();
  ldu(0);
  waitv<1>();
  asm volatile("" : "+v"(pa), "+v"(pi), "+v"(pcr), "+v"(pci), "+v"(pdt), "+v"(pds), "+v"(o5), "+v"(o6), "+v"(o7));
  const float ldt = bf2f(pdt), dsk = bf2f(pds);
  w128(wv + o6[3], u32x4{0, 0, 0, 0});
  lp fw[4] = {wv + o5[0], wv + o5[1], wv + o5[2], wv + o5[3]};
  lp e0 = wv + o6[0], e1 = wv + o6[1], e2 = wv + o6[2];
  lp ua = wv + o7[0], ta = wv + o7[1], t2 = wv + o7[2], fa = wv + o7[3], pw = wv + STG + lane * 8;
  CFENCE();
  STAMP(1);
  const float dt = rbf(__expf(ldt));
  f32x2 lr = {-rbf(__expf(bf2f(pa))) * dt, -rbf(__expf(bfhi(pa))) * dt};
  f32x2 rv = {bf2f(pi) * dt, bfhi(pi) * dt};
  lr *= L2E; rv *= 0.15915494309f;
  const cx2 w1 = cpow(lr, rv, 1.f), wb = cpow(lr, rv, 128.f);
  cx2 w8 = csq(csq(csq(w1)));
  w8.i = -w8.i;
  const cx2 cc = {f32x2{bf2f(pcr), bfhi(pcr)} * (2.f * dt), f32x2{bf2f(pci), bfhi(pci)} * (2.f * dt)};
  ldu(1);
  STAMP(2);
  auto put = [&](int row, cx2 v) { w64(pw + row * PIT, u32x2{pk(v.r[0], v.i[0]), pk(v.r[1], v.i[1])}); };
  auto get = [&](u32x4 (&f)[8]) {
    CFENCE();
#pragma unroll
    for (int s = 0; s < 8; ++s) f[s] = r128(fa + s * 64);
    CFENCE();
    SCHED();
  };
  const f32x4 zero = {0.f, 0.f, 0.f, 0.f};
  u32x4 yf[8], xf[8];
  {
    cx2 y = {f32x2{1.f, 1.f}, f32x2{0.f, 0.f}};
    put(0, y); y = w8; put(1, y);
#pragma unroll
    for (int r = 2; r < 16; ++r) { y = cmul(y, w8); put(r, y); if (r == 7) ldu(2); }
    get(yf);
    ldu(3);
  }
  STAMP(9);
  cx2 e[4];
  {
    cx2 v = cc, u8 = cmul(cc, wb);
#pragma unroll
    for (int a = 0; a < 8; ++a) {
      v = cmul(v, w1); u8 = cmul(u8, w1);
      put(a, v); put(8 + a, u8);
      if (a & 1) e[a >> 1] = u8;
    }
    get(xf);
  }
  STAMP(5);
  u32x4 xg[8], xh[8];
#pragma unroll
  for (int n = 0; n < 8; ++n) { e[n & 3] = cmul(e[n & 3], wb); put(n, e[n & 3]); }
  put(8, cc);
  get(xg);
#pragma unroll
  for (int b = 0; b < 4; ++b) {
    e[1] = cmul(e[1], wb); e[3] = cmul(e[3], wb);
    put(2 * b, e[1]); put(2 * b + 1, e[3]);
  }
#pragma unroll
  for (int n = 0; n < 8; ++n) { e[3] = cmul(e[3], wb); put(8 + n, e[3]); }
  get(xh);
  f32x4 k0 = zero, k1 = zero, k2 = zero;
#pragma unroll
  for (int n = 0; n < 8; ++n) { k0 = mfma(xf[n], yf[n], k0); k1 = mfma(xg[n], yf[n], k1); }
  SCHED();
#pragma unroll
  for (int n = 0; n < 8; ++n) k2 = mfma(xh[n], yf[n], k2);
  const u32 d32 = pk(k0[3], k0[2]);
  {
    const u32 d10 = pk(k0[1], k0[0]), d21 = pk(k0[2], k0[1]);
    w64(e0, u32x2{d32, d10});
    w16(e0 + TPD + 2, d32); CFENCE(); w32(e0 + TPD + 4, d21); CFENCE(); w16(e0 + TPD + 8, d10 >> 16);
  }
  {
    const u32 c33 = pk(k1[3], k1[3]), c32 = pk(k1[3], k1[2]), c11 = pk(k1[1], k1[1]), c10 = pk(k1[1], k1[0]), c21 = pk(k1[2], k1[1]);
    w128(e1 + TPD + 2, u32x4{c33, c32, c11, c10});
    w16(e1, c33); CFENCE(); w32(e1 + 2, c33); CFENCE(); w32(e1 + 6, c21); CFENCE(); w32(e1 + 10, c11); CFENCE(); w16(e1 + 14, c10 >> 16);
    u32 v = pk(k1[0] + dsk, 0.f);
    PIN(v);
    v = lane < 32 ? c33 : lane < 48 ? v : d32;
    w16(e2, v); w16(e2 + TPD + 2, v);
  }
  STAMP(3);
  u32x4 BX[5];
#pragma unroll
  for (int k = 0; k < 5; ++k) BX[k] = tab[(k - 2) * 64];
  const u32 bo = (lane & 16) * 32 + (lane & 12) * 32 + (lane & 32) * 2 + (lane & 3) * 16;
  {
    const u32 f01 = pk(k2[0], k2[1]), f23 = pk(k2[2], k2[3]);
#pragma unroll
    for (int j = 0; j < 4; ++j) { const u32 v = (j < 2 ? f01 : f23) >> (16 * (j & 1)); w16(fw[j], v); w16(fw[j] + FCP + 2, v); }
  }
  const u32 k1024 = __builtin_amdgcn_readlane(pk(k2[3], k2[3]), 31);
  CFENCE();
  u32x4 T[10];
#pragma unroll
  for (int n = 0; n < 20; ++n) {
    const int k = n >> 1, hf = n & 1;
    const int o = k < 3 ? hf * F2S + 64 - 32 * k : k < 6 ? (2 + hf) * F2S + 64 - 32 * (k - 3) : 4 * F2S + hf * F3S + 96 - 32 * (k - 6);
    T[k][2 * hf] = r32(t2 + o); T[k][2 * hf + 1] = r32(t2 + o + 4);
  }
  const u32x4 F32 = r4x32(ta);
  STAMP(4);
#pragma unroll
  for (int c = 0; c < 4; ++c) {
    w64(L + (2 * c) * WVB + 512 + tid * 8, u32x2{__builtin_amdgcn_perm(ur[1][c], ur[0][c], 0x05040100), __builtin_amdgcn_perm(ur[3][c], ur[2][c], 0x05040100)});
    w64(L + (2 * c + 1) * WVB + 512 + tid * 8, u32x2{__builtin_amdgcn_perm(ur[1][c], ur[0][c], 0x07060302), __builtin_amdgcn_perm(ur[3][c], ur[2][c], 0x07060302)});
  }
  WAITL0();
  BAR();
  STAMP(6);
  u32x4 U[2][8], Fa[2], Fb[2];
  auto rdn = [&](int d0, int n) {
    const int q = d0 & 1;
    if (n == 0) { Fa[q][0] = r32(ta + 1024 - d0 * 64); Fa[q][1] = r32(ta + 1028 - d0 * 64); }
    else if (n == 1) { Fa[q][2] = r32(ta + 1032 - d0 * 64); Fa[q][3] = r32(ta + 1036 - d0 * 64); }
    else if (n < 10) U[q][n - 2] = r128(ua + 480 + (n - 2) * 512 - d0 * 64);
    else if (n == 10) { Fb[q][0] = r32(ta + 512 - d0 * 64); Fb[q][1] = r32(ta + 516 - d0 * 64); }
    else { Fb[q][2] = r32(ta + 520 - d0 * 64); Fb[q][3] = r32(ta + 524 - d0 * 64); }
  };
#pragma unroll
  for (int n = 0; n < 12; ++n) rdn(7, n);
  SCHED();
  u32x4 W1[6];
  f32x4 acc[8];
#pragma unroll
  for (int d0 = 7; d0 >= 0; --d0) {
    const int q = d0 & 1;
#pragma unroll
    for (int m = 0; m < 15; ++m) {
      if (d0 < 7 && m == 0) __builtin_amdgcn_s_waitcnt(0xC67F);
      if (d0 < 7 && m == 4) __builtin_amdgcn_s_waitcnt(0xC47F);
      if (m >= 8) acc[m - 7] = mfma(Fb[q], U[q][m - 8], acc[m - 7]);
      else acc[m] = mfma(Fa[q], U[q][m], d0 == 7 ? zero : acc[m]);
      SCHED();
      if (d0 > 0) { if (m < 12) rdn(d0 - 1, m); }
      else if (m < 6) W1[m] = r128(ua + 464 + m * 512);
      SCHED();
    }
  }
  STAMP(7);
  {
    const u32x4 FX = BX[4] & k1024;
    f32x4 s[6];
    u32x4 SA, SB, SC;
    auto box = [&](int b) {
      s[0] = mfma(U[0][b], BX[0], zero); s[1] = mfma(U[0][b], BX[1], zero);
      s[2] = mfma(W1[b], BX[0], zero); s[3] = mfma(W1[b], BX[1], zero);
      if (b < 4) { s[4] = mfma(U[0][b], BX[2], zero); s[5] = mfma(W1[b], BX[3], zero); }
    };
    box(0);
#pragma unroll
    for (int b = 0; b < 6; ++b) {
      acc[b + 2] = mfma(F32, U[0][b], acc[b + 2]);
      if (b < 4) acc[b + 4] = mfma(FX, U[0][b], acc[b + 4]);
      SA = pack2(s[0], s[1]); SB = pack2(s[2], s[3]);
      if (b < 4) SC = pack2(s[4], s[5]);
      if (b < 5) box(b + 1);
#pragma unroll
      for (int d = 0; d < 3; ++d)
        if (b + 2 + d < 8) acc[b + 2 + d] = mfma(SA, T[d], acc[b + 2 + d]);
#pragma unroll
      for (int d = 0; d < 3; ++d)
        if (b + 2 + d < 8) acc[b + 2 + d] = mfma(SB, T[3 + d], acc[b + 2 + d]);
      if (b < 4) {
#pragma unroll
        for (int d = 0; d < 4; ++d)
          if (b + 4 + d < 8) acc[b + 4 + d] = mfma(SC, T[6 + d], acc[b + 4 + d]);
      }
    }
#pragma unroll
    for (int i = 0; i < 76; ++i) { __builtin_amdgcn_sched_group_barrier(0x8, 1, 0); __builtin_amdgcn_sched_group_barrier(0x2, 1, 0); }
  }
  STAMP(8);
  const rsrc_t rs = wsrsrc(ws);
  const u32 dst = WS_BT + chn * BTP + bo;
#pragma unroll
  for (int i = 0; i < 8; i += 2) {
    auto sx = __builtin_amdgcn_permlane16_swap(pk(acc[i][0], acc[i][1]), pk(acc[i + 1][0], acc[i + 1][1]), false, false);
    auto sy = __builtin_amdgcn_permlane16_swap(pk(acc[i][2], acc[i][3]), pk(acc[i + 1][2], acc[i + 1][3]), false, false);
    st_sc1(rs, dst + i * 512, u32x4{sx[0], sy[0], sx[1], sy[1]});
  }
}

extern "C" __global__ __launch_bounds__(512, 2) void gm_k1(const char* x, const char* w, const char* wo, const u32* logA, const u32* Aim,
                                             const u32* Cre, const u32* Cim, const u16* logdt, const u16* Dsk, const char* gw, char* ws, const char* out) {
  extern __shared__ __attribute__((aligned(16))) char smem[];
  lp L = (lp)smem;
  u64 pad0; asm volatile("s_memrealtime %0" : "=s"(pad0));
  SCHED();
  STDECL;
  STAMP(0);
  const int xcd = blockIdx.x & 7, loc = (int)(blockIdx.x >> 3) - 1;
  const int wave = __builtin_amdgcn_readfirstlane(threadIdx.x >> 6), lane = threadIdx.x & 63;
  if (__builtin_expect(loc < 0, 0)) {
    const char* pc; u32 back, len;
    asm volatile("s_getpc_b64 %0\n1:\n\ts_mov_b32 %1, 1b-gm_k1\n\ts_mov_b32 %2, gm_code_end-gm_k1" : "=s"(pc), "=s"(back), "=s"(len));
    pc -= back;
    touch(pc, min((u32)threadIdx.x * 128, len));
    if (wave < 3) touch(ws + WS_EP, min(threadIdx.x * 128, 175u * 128));
    if (wave == 7) { touch(ws, min(lane * 131072, WS_END - 4)); touch(out, min(lane * 65536, SEQ * XROW - 4)); }
    const char* wsrc = wo + (xcd & 1) * (16 * 18 * XROW);
    u32x4 gv[9];
#pragma unroll
    for (int i = 0; i < 9; ++i) gv[i] = *(const u32x4*)(gw + i * 128 + (lane & 7) * 16);
    float res = 0.f;
#pragma unroll
    for (int p = 0; p < 4; ++p) {
      const char* xp = x + (u32)(xcd * 256 + wave * 32 + (lane >> 3) * 4 + p) * XROW + (lane & 7) * 16;
      u32x4 xv[9];
#pragma unroll
      for (int i = 0; i < 9; ++i) xv[i] = *(const u32x4*)(xp + i * 128);
      float a = 0.f;
#pragma unroll
      for (int i = 0; i < 9; ++i)
#pragma unroll
        for (int d = 0; d < 4; ++d) {
          const u32 xa = xv[i][d], ga = gv[i][d];
          a = __builtin_amdgcn_fdot2_f32_bf16(__builtin_bit_cast(bf16x2, xa), __builtin_bit_cast(bf16x2, ga), a, false);
        }
      a += __builtin_bit_cast(float, __builtin_amdgcn_update_dpp(0, __builtin_bit_cast(int, a), 0xb1, 0xf, 0xf, false));
      a += __builtin_bit_cast(float, __builtin_amdgcn_update_dpp(0, __builtin_bit_cast(int, a), 0x4e, 0xf, 0xf, false));
      a += __builtin_bit_cast(float, __builtin_amdgcn_update_dpp(0, __builtin_bit_cast(int, a), 0x141, 0xf, 0xf, false));
      res = (lane & 3) == p ? a : res;
    }
    res = rbf(__builtin_amdgcn_rcpf(1.f + __builtin_amdgcn_exp2f(-L2E * rbf(res))));
    res = __int_as_float(__builtin_amdgcn_ds_bpermute(((lane >> 2) * 8 + (lane & 3)) * 4, __float_as_int(res)));
    st32_sc1(wsrsrc(ws), lane < 32 ? WS_G + (xcd * 256 + wave * 32 + lane) * 4 : OOR, __float_as_uint(res));
    while (__builtin_amdgcn_s_memrealtime() - pad0 < PAD1) __builtin_amdgcn_s_sleep(4);
    return;
  }
  const bool isq = loc >= 25;
  const int Q = xcd >> 1, l = loc - 25;
  const int rq = ((0x8530 >> (4 * Q)) & 15) + (l >> 1), rk = Q * 4 + (loc >> 2);
  const int cq = (xcd & 1) * 2 + (l & 1), ck = (xcd & 1) * 4 + (loc & 3);
  const int sq = 3 * (l >> 1) + 4 * (l & 1) + 1, sk = 2 * ((loc >> 2) + (loc & 3));
  int rbi = isq ? rq : rk, cg = isq ? cq : ck, c0 = isq ? sq : sk;
  c0 = c0 >= 9 ? c0 - 9 : c0;
  asm volatile("" : "+s"(rbi), "+s"(cg), "+s"(c0));
  PINS(x); PINS(w); PINS(wo); PINS(logA); PINS(Aim); PINS(Cre); PINS(Cim); PINS(logdt); PINS(Dsk); PINS(gw); PINS(ws);
  if (Q == 1 && l >= 4) return;
  if (__builtin_expect((loc < 16 || isq) && wave < 4, 1)) {
    role_load(L, x, w, wo, ws, isq, rbi, cg, c0, wave, lane STPASS);
  } else if (loc < 16) {
    role_kv(L, ws, rbi, cg, wave - 4, lane STPASS);
  } else if (isq) {
    role_q(L, ws, rbi, cg, wave - 4, lane STPASS);
  } else {
    role_ssm(L, x, logA, Aim, Cre, Cim, logdt, Dsk, ws, xcd * 9 + loc - 16, wave, lane STPASS);
  }
  STFLUSH(0);
}

constexpr int K2S = 27648, K2B = 5 * K2S, K2G = K2B + 10240;
template <int NR>
DI void k2_compute(lp L, char* out, int rt0, int r0, int cb, int ch, int lane STARG) {
  const int fr = lane & 15, fh = lane >> 4;
  const int col = 32 * ch + 2 * fr;
  lp B0 = L + fh * 1024 + ((col ^ (col >> 4)) << 4), B1 = L + fh * 1024 + (((col + 1) ^ (col >> 4)) << 4);
  lp A = L + 12288 + r0 * 3072 + lane * 16;
  lp B0h = B0 + 2 * K2S, B1h = B1 + 2 * K2S, Ah = A + 2 * K2S;
  lp Be = L + fh * 256 + col * 4, Ae = L + 12288 + r0 * 3072 + (lane & 31) * 16 + (lane >> 5) * 8;
  lp Bb = L + K2B + (32 * ch + fr) * 160 + ((fh >> 1) * 5 + r0) * 16 + (fh & 1) * 8, Gp = L + K2G + r0 * 64 + fh * 16;
  u32 dst = ((rt0 + r0) * 16 + fh * 4) * XROW + cb * 128 + col * 2;
  PIN(B0); PIN(B1); PIN(A); PIN(B0h); PIN(B1h); PIN(Ah); PIN(Be); PIN(Ae); PIN(Bb); PIN(Gp); PIN(dst); PINS(out);
  f32x4 acc[NR][2], cst[NR][2], g[NR];
  u32x4 a[2][NR], b[2][2];
  auto rd = [&](int n, int k) {
    const int sl = n / 3, s = n % 3, q = n & 1;
    if (k > NR + 1) return;
    const int t = k == 0 ? 0 : 1, i = k - 1;
    const bool isb = k == 0 || k == NR + 1;
    if (sl == 0 && s > 0) {
      if (isb) { lp e = Be + s * 4096 + t * 4; b[q][t] = u32x4{r32(e), r32(e + 1024), r32(e + 2048), r32(e + 3072)}; }
      else {
        const u32x2 lo = r64(Ae + i * 3072 + s * 1024), hi = r64(Ae + i * 3072 + s * 1024 + 512);
        a[q][i] = u32x4{lo[0], lo[1], hi[0], hi[1]};
      }
    } else {
      const bool up = sl >= 2 && sl < 5;
      const int o = (sl == 5 ? 0 : up ? sl - 2 : sl) * K2S;
      if (isb) b[q][t] = r128((t ? (up ? B1h : B1) : (up ? B0h : B0)) + o + s * 4096);
      else a[q][i] = r128((up ? Ah : A) + o + i * 3072 + s * 1024);
    }
    SCHED();
  };
  BAR();
  STAMP(1);
#pragma unroll
  for (int k = 0; k < NR + 2; ++k) rd(0, k);
#pragma unroll
  for (int n = 0; n < 18; ++n) {
    if (n % 3 == 2 && n < 17) { BAR(); STAMP(2 + n / 3); }
    if (n < 17) { rd(n + 1, 0); rd(n + 1, 1); }
#pragma unroll
    for (int m = 0; m < 2 * NR; ++m) {
      const int t = m / NR, i = m % NR;
      acc[i][t] = mfma(a[n & 1][i], b[n & 1][t], n ? acc[i][t] : f32x4{0.f, 0.f, 0.f, 0.f});
      SCHED();
      if (n < 17) rd(n + 1, m + 2);
    }
    if (n == CSTN) {
      u32x2 bv[NR][2];
#pragma unroll
      for (int i = 0; i < NR; ++i) {
        g[i] = __builtin_bit_cast(f32x4, r128(Gp + i * 64));
        bv[i][0] = r64(Bb + i * 16); SCHED(); bv[i][1] = r64(Bb + 2560 + i * 16); SCHED();
      }
      SCHED();
#pragma unroll
      for (int i = 0; i < NR; ++i)
#pragma unroll
        for (int t = 0; t < 2; ++t) {
          cst[i][t] = f32x4{bf2f(bv[i][t][0]), bfhi(bv[i][t][0]), bf2f(bv[i][t][1]), bfhi(bv[i][t][1])} * (1.f - g[i]);
          PIN(cst[i][t]);
        }
      SCHED();
    }
  }
  STAMP(7);
  const rsrc_t ro = __builtin_amdgcn_make_buffer_rsrc(out, 0, SEQ * XROW, 0x00020000);
#pragma unroll
  for (int i = 0; i < NR; ++i) {
    f32x4 u = acc[i][0] * g[i] + cst[i][0], v = acc[i][1] * g[i] + cst[i][1];
    PIN(u); PIN(v);
#pragma unroll
    for (int j = 0; j < 4; ++j) __builtin_amdgcn_raw_buffer_store_b32(pk(u[j], v[j]), ro, dst + j * XROW, 16 * i * XROW, AUX_OUT);
  }
}

extern "C" __global__ __launch_bounds__(512, 2) void gm_k2(const char* ws, char* out) {
  extern __shared__ __attribute__((aligned(16))) char smem[];
  lp L = (lp)smem;
  u64 pad0; asm volatile("s_memrealtime %0" : "=s"(pad0));
  SCHED();
  STDECL;
  STAMP(0);
  const int xcd = blockIdx.x & 7, id = xcd * 29 + (xcd < 2 ? xcd : 2) + (blockIdx.x >> 3);
  const int rb = (id * 7282) >> 16, cb = id - rb * 9;
  const int wave = __builtin_amdgcn_readfirstlane(threadIdx.x >> 6), lane = threadIdx.x & 63;
  if (wave < 4) {
    __builtin_amdgcn_s_setprio(2);
    const int t1 = min(5 * rb + wave, 127), t4 = min(5 * rb + 4, 127);
    const u32 vm = WS_M + cb * 73728 + wave * 3072 + lane * 16;
    const u32 v1 = WS_PQ + t1 * 18432 + lane * 16, v4 = WS_PQ + t4 * 18432 + wave * 768 + lane * 16;
    auto issue = [&](int sl) {
      const u32 gq = sl == 0 ? 5 : sl - 1;
      const u32 lb = (sl == 5 ? 0 : sl) * K2S;
      const char* pb = ws + gq * 3072;
      glds3(ws + gq * 12288, lb + wave * 3072, vm);
      glds3(pb, lb + 12288 + wave * 3072, v1);
      asm volatile("s_mov_b32 m0, %2\n\ts_mov_b32 exec_hi, 0xffff\n\tglobal_load_lds_dwordx4 %0, %1\n\ts_mov_b32 exec_hi, -1"
                   ::"v"(v4), "s"(pb), "s"(lb + 24576 + wave * 768) : "memory");
    };
    STAMP(8);
    issue(0); issue(1); issue(2);
    SCHED();
    u32 bo[3];
#pragma unroll
    for (int i = 0; i < 3; ++i) {
      const int f = i * 64 + (i < 2 ? lane : lane & 31);
      const int ir = (f * 6554) >> 16, pc = f - ir * 10, hh = pc >= 5;
      const int rt = min(5 * rb + pc - 5 * hh, 127);
      bo[i] = ir * (2 * BTP) + (rt >> 2) * 128 + hh * 64 + (rt & 3) * 16 - (i == 1 ? 1024 : 0);
      PIN(bo[i]);
    }
    const char* bt = ws + (WS_BT + (cb * 64 + (wave >> 1) * 32 + (wave & 1)) * BTP);
    STAMP(9);
    waitv<14>(); BAR();
    issue(3);
    waitv<14>(); BAR();
    if (wave == 3 && lane < 20) glds(ws + WS_G, min(rb * 320 + lane * 16, SEQ * 4 - 16), K2G);
    glds2(bt, K2B + wave * 2560, bo[0], bo[1]);
    glds_lo(bt, bo[2], K2B + wave * 2560 + 2048);
    issue(4); waitv<17>(); BAR();
    issue(5); waitv<17>(); BAR();
    waitv<7>(); BAR();
    waitv<0>(); BAR();
    __builtin_amdgcn_s_setprio(0);
    while (__builtin_amdgcn_s_memrealtime() - pad0 < PAD2) __builtin_amdgcn_s_sleep(4);
  } else {
    if (wave < 6) k2_compute<3>(L, out, 5 * rb, 0, cb, wave & 1, lane STPASS);
    else k2_compute<2>(L, out, 5 * rb, 3, cb, wave & 1, lane STPASS);
  }
  STFLUSH(1);
}

extern "C" __device__ __attribute__((used, noinline)) void gm_code_end() {}

extern "C" void kernel_launch(void* const* d_in, const int* in_sizes, int n_in, void* d_out, int out_size, void* d_ws,
                              size_t ws_size, hipStream_t stream) {
  (void)hipFuncSetAttribute((const void*)gm_k1, hipFuncAttributeMaxDynamicSharedMemorySize, K1_LDS);
  (void)hipFuncSetAttribute((const void*)gm_k2, hipFuncAttributeMaxDynamicSharedMemorySize, K2_LDS);
  gm_k1<<<256, 512, K1_LDS, stream>>>((const char*)d_in[0], (const char*)d_in[1], (const char*)d_in[2], (const u32*)d_in[4],
                                   (const u32*)d_in[5], (const u32*)d_in[6], (const u32*)d_in[7], (const u16*)d_in[8],
                                   (const u16*)d_in[9], (const char*)d_in[10], (char*)d_ws, (const char*)d_out);
  gm_k2<<<234, 512, K2_LDS, stream>>>((const char*)d_ws, (char*)d_out);
}
```

```cpp
#include <hip/hip_runtime.h>
#include <stdint.h>

typedef unsigned int u32;
typedef unsigned short u16;
typedef unsigned long long u64;
typedef __attribute__((ext_vector_type(2))) unsigned u32x2;
typedef __attribute__((ext_vector_type(4))) unsigned u32x4;
typedef __attribute__((ext_vector_type(2))) float f32x2;
typedef __attribute__((ext_vector_type(4))) float f32x4;
typedef __attribute__((ext_vector_type(4))) short s16x4;
typedef __attribute__((ext_vector_type(2))) __bf16 bf16x2;
typedef __attribute__((ext_vector_type(8))) __bf16 bf16x8;
#define LAS __attribute__((address_space(3)))
#define GAS __attribute__((address_space(1)))
#define DI __device__ __forceinline__
typedef LAS char* lp;

constexpr int SEQ = 2048, DM = 576;
constexpr int XROW = DM * 2;
constexpr int WROW = 3 * DM * 2;
constexpr int BTP = 4224;
constexpr int FLS = 128;
constexpr int AUX_OUT = 16;
constexpr int ZPOS = 16;
constexpr int BRD = 13;
constexpr int M_DELAY = 3;
constexpr int PQ_DELAY = 1;
constexpr int PQ_SLEEP = 3;
constexpr int POLL_SLEEP = 2;
constexpr int WS_PQ = 0;
constexpr int WS_M = WS_PQ + 128 * 18 * 1024;
constexpr int WS_BT = WS_M + 36 * 18 * 1024;
constexpr int WS_G = WS_BT + DM * BTP;
constexpr int WS_MBKV = WS_G + SEQ * 4;
constexpr int MBKV_SZ = 16 * 640;
constexpr int WS_ZC = WS_MBKV + 128 * MBKV_SZ;
constexpr int WS_EP = WS_ZC + 4096;
constexpr int WS_FL = WS_EP + 128;
constexpr int FL_M = 0, FL_PQ = 96, FL_BT = 148, FL_G = 220, FL_N = 224;
constexpr int WS_END = WS_FL + FL_N * FLS;
constexpr int K1_LDS = 162304;

DI float bf2f(u32 v) { return __uint_as_float(v << 16); }
DI float bfhi(u32 v) { return __uint_as_float(v & 0xffff0000u); }
DI u32 pk(float a, float b) { bf16x2 v = {(__bf16)a, (__bf16)b}; return __builtin_bit_cast(u32, v); }
DI float rbf(float a) { return bf2f(pk(a, a) & 0xffffu); }
DI f32x4 mfma(u32x4 a, u32x4 b, f32x4 c) {
  return __builtin_amdgcn_mfma_f32_16x16x32_bf16(__builtin_bit_cast(bf16x8, a), __builtin_bit_cast(bf16x8, b), c, 0, 0, 0);
}
DI u32x4 r128(lp a) { return *(LAS u32x4*)a; }
DI u32x2 r64(lp a) { return *(LAS u32x2*)a; }
DI u32 r32(lp a) { return *(LAS u32*)a; }
DI void w128(lp a, u32x4 v) { *(LAS u32x4*)a = v; }
DI void w64(lp a, u32x2 v) { *(LAS u32x2*)a = v; }
DI void w32(lp a, u32 v) { *(LAS u32*)a = v; }
DI void w16(lp a, u32 v) { *(LAS u16*)a = (u16)v; }
DI u32x2 rtr(lp a) { return __builtin_bit_cast(u32x2, __builtin_amdgcn_ds_read_tr16_b64_v4i16((LAS s16x4*)a)); }
DI u32x4 rtr2(lp a0, lp a1) { u32x2 l = rtr(a0), h = rtr(a1); return u32x4{l[0], l[1], h[0], h[1]}; }
DI u32x4 r4x32(lp a) { return u32x4{r32(a), r32(a + 4), r32(a + 8), r32(a + 12)}; }
DI void glds(const void* sbase, u32 voff, u32 lds) {
  asm volatile("s_mov_b32 m0, %2\n\ts_nop 3\n\tglobal_load_lds_dwordx4 %0, %1" ::"v"(voff), "s"(sbase), "s"(lds) : "memory");
}
DI void glds4(const void* sbase, u32 lds, u32 v0, u32 v1, u32 v2, u32 v3) {
  asm volatile("s_mov_b32 m0, %5\n\ts_nop 3\n\tglobal_load_lds_dwordx4 %0, %4\n\tglobal_load_lds_dwordx4 %1, %4 offset:1024\n\t"
               "global_load_lds_dwordx4 %2, %4 offset:2048\n\tglobal_load_lds_dwordx4 %3, %4 offset:3072"
               ::"v"(v0), "v"(v1), "v"(v2), "v"(v3), "s"(sbase), "s"(lds) : "memory");
}
DI void glds45s(const void* sbase, u32 lds, u32 v) {
  asm volatile("s_mov_b32 m0, %2\n\ts_mov_b32 exec_hi, 0\n\ts_nop 2\n\tglobal_load_lds_dwordx4 %0, %1 sc1\n\ts_mov_b32 exec_hi, -1\n\t"
               "global_load_lds_dwordx4 %0, %1 offset:512 sc1\n\tglobal_load_lds_dwordx4 %0, %1 offset:1536 sc1\n\t"
               "global_load_lds_dwordx4 %0, %1 offset:2560 sc1\n\tglobal_load_lds_dwordx4 %0, %1 offset:3584 sc1"
               ::"v"(v), "s"(sbase), "s"(lds) : "memory");
}
DI void glds_lo(const void* sbase, u32 voff, u32 lds) {
  asm volatile("s_mov_b32 m0, %2\n\ts_mov_b32 exec_hi, 0\n\ts_nop 2\n\tglobal_load_lds_dwordx4 %0, %1\n\ts_mov_b32 exec_hi, -1"
               ::"v"(voff), "s"(sbase), "s"(lds) : "memory");
}
DI void touch(const void* sbase, u32 voff) {
  asm volatile("s_mov_b32 m0, %2\n\ts_nop 3\n\tglobal_load_lds_dword %0, %1 sc1" ::"v"(voff), "s"(sbase), "s"(K1_LDS - 256) : "memory");
}
template <int N> DI void waitv() { asm volatile("s_waitcnt vmcnt(%0)" ::"i"(N) : "memory"); }
#define CFENCE() asm volatile("" ::: "memory")
#define WAITL0() do { __builtin_amdgcn_s_waitcnt(0xC07F); CFENCE(); } while (0)
#define BAR() do { CFENCE(); __builtin_amdgcn_s_barrier(); CFENCE(); } while (0)
#define SCHED() __builtin_amdgcn_sched_barrier(0)
#define PIN(x) asm volatile("" : "+v"(x))
#define PINS(x) asm volatile("" ::"s"(x))
constexpr float EPS = 1e-6f, L2E = 1.44269504089f;
DI f32x2 phi2(float a0, float a1) {
  f32x2 m = f32x2{a0, a1} * L2E - EPS * L2E;
  f32x2 e = {__builtin_amdgcn_fmed3f(__builtin_amdgcn_exp2f(m[0]), 0.f, 1.f), __builtin_amdgcn_fmed3f(__builtin_amdgcn_exp2f(m[1]), 0.f, 1.f)};
  const int ei = __builtin_bit_cast(int, EPS);
  f32x2 x = {__int_as_float(max(__float_as_int(a0), ei)), __int_as_float(max(__float_as_int(a1), ei))};
  return x + e;
}
DI f32x4 phi4(f32x4 a) { f32x2 l = phi2(a[0], a[1]), h = phi2(a[2], a[3]); return f32x4{l[0], l[1], h[0], h[1]}; }
DI float prodsum(float a, float b, float c, float d) {
  auto s0 = __builtin_amdgcn_permlane32_swap(__float_as_uint(a), __float_as_uint(c), false, false);
  auto s1 = __builtin_amdgcn_permlane32_swap(__float_as_uint(b), __float_as_uint(d), false, false);
  const float ac = __uint_as_float(s0[0]) + __uint_as_float(s0[1]), bd = __uint_as_float(s1[0]) + __uint_as_float(s1[1]);
  auto s2 = __builtin_amdgcn_permlane16_swap(__float_as_uint(ac), __float_as_uint(bd), false, false);
  float v = __uint_as_float(s2[0]) + __uint_as_float(s2[1]);
  v += __builtin_bit_cast(float, __builtin_amdgcn_update_dpp(0, __builtin_bit_cast(int, v), 0x128, 0xf, 0xf, false));
  v += __builtin_bit_cast(float, __builtin_amdgcn_update_dpp(0, __builtin_bit_cast(int, v), 0x124, 0xf, 0xf, false));
  return v;
}
DI float prodsum(u32x4 m) { return prodsum(bf2f(m[0]), bfhi(m[0]), bf2f(m[2]), bfhi(m[2])); }
DI u32x4 pack2(f32x4 a, f32x4 b) { return u32x4{pk(a[0], a[1]), pk(a[2], a[3]), pk(b[0], b[1]), pk(b[2], b[3])}; }
typedef __amdgpu_buffer_rsrc_t rsrc_t;
DI rsrc_t wsrsrc(char* ws) { return __builtin_amdgcn_make_buffer_rsrc(ws, 0, WS_END, 0x00020000); }
DI void st_sc1(rsrc_t r, u32 off, u32x4 v) { __builtin_amdgcn_raw_buffer_store_b128(v, r, off, 0, 16); }
DI u32x4 ld_sc1(rsrc_t r, u32 off) { return __builtin_amdgcn_raw_buffer_load_b128(r, off, 0, 16); }
DI void st32_sc1(rsrc_t r, u32 off, u32 v) { __builtin_amdgcn_raw_buffer_store_b32(v, r, off, 0, 16); }
DI u32 ld_tag(rsrc_t r) { return __builtin_amdgcn_raw_buffer_load_b32(r, 0, WS_EP, 16) + 1; }
DI void st_flag(rsrc_t r, int i, int lane, u32 v) { if (lane < FLS / 16) st_sc1(r, WS_FL + FLS * i + 16 * lane, u32x4{v, v, v, v}); }
constexpr u32 OOR = 0x7ffffff0u;
constexpr u32 SPIN_CAP = 1u << 15;
template <int SLEEP> DI void wait_flags(rsrc_t r, u32 off, u32 tag) {
  for (u32 spin = 0; spin < SPIN_CAP; ++spin) {
    CFENCE();
    const u32 v = __builtin_amdgcn_raw_buffer_load_b32(r, off, 0, 16);
    off = v == tag ? OOR : off;
    if (__all(off == OOR)) break;
    __builtin_amdgcn_s_sleep(SLEEP);
  }
  CFENCE();
}
DI u32 lds_arrive(lp p, int lane) {
  CFENCE();
  u32 old = 0;
  if (lane == 0) old = __hip_atomic_fetch_add((LAS u32*)p, 1u, __ATOMIC_RELAXED, __HIP_MEMORY_SCOPE_WORKGROUP);
  CFENCE();
  return __builtin_amdgcn_readfirstlane(old);
}
DI void lds_wait(lp p, u32 n) {
  for (u32 spin = 0; spin < (1u << 20); ++spin) {
    CFENCE();
    if (__builtin_amdgcn_readfirstlane(r32(p)) >= n) break;
    __builtin_amdgcn_s_sleep(1);
  }
  CFENCE();
}
DI void lds_meet4(lp p, int lane) {
  u32 v = lds_arrive(p, lane) + 1;
  for (u32 spin = 0; v != 4 && spin < (1u << 22); ++spin) { CFENCE(); v = __builtin_amdgcn_readfirstlane(r32(p)); }
  CFENCE();
}

constexpr int XRING = 4 * 18432;
constexpr int WOI = XRING + 4 * 16384;
constexpr int GWL = WOI + 21248, CNT = GWL + 1152;
constexpr int XCH = XRING + 16384, KVA = 18432;
static_assert(XRING + 4 * 20480 <= GWL && GWL + 1280 <= K1_LDS - 256, "lds");

constexpr u32 POISON = 0xffffffffu;
template <int ROLE, class F>
DI void gemm_main(lp L, int w, int lane, f32x4 (&acc)[ROLE ? 5 : 2][ROLE ? 5 : 9], F before_last) {
  constexpr int RT = ROLE ? 5 : 2, CT = ROLE ? 5 : 9, XB = ROLE ? 20480 : 16384, NM = ROLE ? 23 : 18;
  const int fr = lane & 15, fh = lane >> 4;
  const int q4 = (lane >> 2) & 3, p4 = lane & 3;
  const int ax = XRING + fr * 128 + ((fh ^ ((fr >> 1) & 7)) << 4);
  const int ta = ROLE ? (w >> 1) * 5 + (w & 1) * 2 : w * 2, tb = (w >> 1) * 5 + 3 - (w & 1) * 3;
  const int r0 = (8 * fh + q4 + 4 * (fh & 1)) * 288, r1 = (8 * fh + q4 + 4 * ((fh & 1) ^ 1)) * 288;
  const int cm = ROLE ? 72 * p4 + 40 * (w & 1) : 8 * p4, cx = 72 * p4 + 32;
  u32x4 a0[RT], a1[RT], b0[CT], b1[CT];
  lp W0 = L + r0 + cm, W1 = L + r1 + cm, E0 = L + r0 + cx, E1 = L + r1 + cx;
  lp X0 = L + ax + ta * 2048, X1 = L + (ax ^ 64) + ta * 2048, Y0 = L + ax + tb * 2048, Y1 = L + (ax ^ 64) + tb * 2048;
  lp X0b = X0 + 4096, X1b = X1 + 4096;
  PIN(E0); PIN(E1);
  if (ROLE) { PIN(Y0); PIN(Y1); PIN(X0b); PIN(X1b); }
  auto mm = [&](int m, u32x4 (&a)[RT], u32x4 (&b)[CT]) {
    const int t = m / RT, i = m % RT;
    acc[i][t] = ROLE ? mfma(b[t], a[i], acc[i][t]) : mfma(a[i], b[t], acc[i][t]);
  };
  auto step = [&](bool domma, u32x4 (&a)[RT], u32x4 (&b)[CT], int slot, int s, u32x4 (&an)[RT], u32x4 (&bn)[CT]) {
    auto rda = [&](int n) {
      an[n] = r128((n < 2 ? (s ? X1 : X0) + n * 2048 : n == 2 ? (s ? X1b : X0b) : (s ? Y1 : Y0) + (n - 3) * 2048) + slot * XB);
    };
    auto rdb = [&](int n) {
      const int t = n >> 1, so = slot * 18432 + s * 9216;
      const int to = (ROLE ? 8 * t : 72 * (t >> 1) + 40 * (t & 1)) + so;
      const u32x2 v = t < CT - 1 ? rtr((n & 1 ? W1 : W0) + to) : rtr((n & 1 ? E1 : E0) + so);
      bn[t][2 * (n & 1)] = v[0]; bn[t][2 * (n & 1) + 1] = v[1];
    };
    auto rd = [&](int n) {
      if (n >= RT + 2 * CT) return;
      if (ROLE) { if (n < 2) rdb(n); else if (n < 7) rda(n - 2); else rdb(n - 5); }
      else { if (n < 2) rda(n); else rdb(n - 2); }
    };
    const int EX = ROLE ? 0 : (s ? 7 : 2);
#pragma unroll
    for (int m = 0; m < NM; ++m) {
      if (domma) mm(m, a, b);
      SCHED();
      if (m < 2 * EX) { rd(m + (m + 1) / 2); if (!(m & 1)) rd(m + m / 2 + 1); } else rd(m + EX);
      SCHED();
    }
  };
#pragma unroll
  for (int m = 0; m < NM; ++m) {
    const int t = m / RT, i = m % RT;
    const float v = (ROLE || t < 4 || (t == 8 && fr < 8)) ? EPS : 0.f;
    acc[i][t] = f32x4{v, v, v, v}; PIN(acc[i][t]);
  }
  BAR();
  step(false, a1, b1, 0, 0, a0, b0);
#pragma unroll 1
  for (int it = 0; it < 2; ++it) {
#pragma unroll
    for (int sl = 0; sl < 4; ++sl) {
      step(true, a0, b0, sl, 1, a1, b1);
      WAITL0();
      BAR();
      SCHED();
      step(true, a1, b1, (sl + 1) & 3, 0, a0, b0);
    }
  }
  before_last();
  step(true, a0, b0, 0, 1, a1, b1);
#pragma unroll
  for (int m = 0; m < NM; ++m) mm(m, a1, b1);
}

DI void m_addr(u32 (&mo)[5], int chead, int crole, int wave, int lane) {
  const int un = 2 * (chead >> 3) + (chead & 1), hh = (chead >> 1) & 3;
#pragma unroll
  for (int i = 0; i < 5; ++i) {
    const int p = wave + 4 * i, cb = (p * 43) >> 7, col = (p - 3 * cb) * 32 + (lane & 31);
    mo[i] = WS_M + cb * 110592 + (crole == 2 ? 98304 + (un * 4 + hh) * 384 + (col ^ (hh & 1)) * 4 : ((2 * un + crole) * 4 + hh) * 1536 + ((col ^ (col >= 48)) << 4));
    if (lane >= 32 || p >= 18) mo[i] = OOR;
  }
}
DI void kv_consume(lp L, rsrc_t rs, int rb, int g, u32 tme, int wave, int lane) {
  const int chead = 4 * g + (rb >> 2), crole = rb & 3;
  if (crole == 3) {
    if (wave) return;
    const u32 off = WS_MBKV + (g * 16 + rb) * MBKV_SZ + (lane >> 2) * 640 + (lane & 3) * 16;
    __builtin_amdgcn_s_setprio(2);
    waitv<0>();
    u32x4 m, e;
    for (u32 spin = 0;; ++spin) {
      CFENCE();
      m = ld_sc1(rs, off); e = ld_sc1(rs, off + 64);
      if (__all(m[1] == tme && m[3] == tme && e[1] == tme && e[3] == tme) || spin > SPIN_CAP) break;
      __builtin_amdgcn_s_sleep(POLL_SLEEP);
    }
    const float sm = prodsum(m), se = prodsum(e);
    const u32 v = __float_as_uint((lane & 4) ? se : sm);
    auto s1 = __builtin_amdgcn_permlane16_swap(v, v, false, false);
    const u32 p = pk(__uint_as_float(s1[0]), __uint_as_float(s1[1]));
    auto s2 = __builtin_amdgcn_permlane32_swap(p, p, false, false);
    st_sc1(rs, lane < 8 ? WS_ZC + chead * 128 + lane * 16 : OOR, u32x4{s2[0], tme, s2[1], tme});
    return;
  }
  const int fr = lane & 15, fh = lane >> 4;
  const bool ext = crole == 2;
  u32x4 wf[10];
  {
    const int codd = chead & 1, q4 = (lane >> 2) & 3, p4 = lane & 3;
    int s0 = 8 * fh + q4, s1 = s0 + 4;
    s0 = s0 > 17 ? 17 : s0; s1 = s1 > 17 ? 17 : s1;
    s0 += 2 * codd; s1 += 2 * codd;
    s0 = s0 >= 18 ? s0 - 18 : s0; s1 = s1 >= 18 ? s1 - 18 : s1;
    lp p0 = L + WOI + s0 * XROW + wave * 64 + p4 * 8, p1 = L + WOI + s1 * XROW + wave * 64 + p4 * 8;
#pragma unroll
    for (int i = 0; i < 10; ++i) wf[i] = rtr2(p0 + (i >> 1) * 256 + (i & 1) * 32, p1 + (i >> 1) * 256 + (i & 1) * 32);
  }
  u32 mo[5];
  m_addr(mo, chead, crole, wave, lane);
#pragma unroll
  for (int i = 0; i < 5; ++i) PIN(mo[i]);
  lp afp = L + KVA + fr * 64 + fh * 16;
  const int it = 4 * wave + (lane & 3);
  const u32 off = WS_MBKV + (g * 16 + rb) * MBKV_SZ + (lane >> 2) * 640 + it * 16;
  const bool keep = fr < 4 && (!ext || (fh >> 1) == (chead & 1));
  lp d0 = L + KVA + (!keep ? 512 + lane * 2 : (ext ? fh & 1 : fh) * 64 + it * 2);
  lp d1 = L + KVA + (!keep || (ext && it > 1) ? 512 + lane * 2 : ext ? (fh & 1) * 64 + 32 + it * 2 : (4 + fh) * 64 + it * 2);
  lp d2 = L + KVA + (fr < 4 ? ((fr >> 1) * 4 + fh) * 64 + 32 + (fr & 1) * 2 : 512 + lane * 2);
  PIN(afp); PIN(d0); PIN(d1); PIN(d2);
  const bool two = !ext || wave == 0, three = !ext && wave == 0;
  __builtin_amdgcn_s_setprio(2);
  waitv<0>();
  u32x4 m, e = {0, tme, 0, tme}, f = e;
  for (u32 spin = 0;; ++spin) {
    CFENCE();
    m = ld_sc1(rs, off);
    if (two) e = ld_sc1(rs, off + 256);
    if (three) f = ld_sc1(rs, off + 512);
    bool ok = m[1] == tme && m[3] == tme && e[1] == tme && e[3] == tme && f[1] == tme && f[3] == tme;
    if (__all(ok) || spin > SPIN_CAP) break;
    __builtin_amdgcn_s_sleep(POLL_SLEEP);
  }
  const u32 sv = pk(prodsum(m), prodsum(e));
  w16(d0, sv); w16(d1, sv >> 16);
  if (three) { const float sf = prodsum(f); w16(d2, pk(sf, sf)); }
  lds_meet4(L + CNT, lane);
  const u32x4 af = r128(afp);
#pragma unroll
  for (int i = 0; i < 5; ++i) {
    const f32x4 u = mfma(af, wf[2 * i], f32x4{0.f, 0.f, 0.f, 0.f}), v = mfma(af, wf[2 * i + 1], f32x4{0.f, 0.f, 0.f, 0.f});
    auto sx = __builtin_amdgcn_permlane16_swap(pk(u[0], u[1]), pk(v[0], v[1]), false, false);
    auto sy = __builtin_amdgcn_permlane16_swap(pk(u[2], u[3]), pk(v[2], v[3]), false, false);
    if (ext) st32_sc1(rs, mo[i], sx[0]);
    else st_sc1(rs, mo[i], u32x4{sx[0], sy[0], sx[1], sy[1]});
  }
  waitv<0>();
  if (lds_arrive(L + CNT + 36, lane) == 3) st_flag(rs, FL_M + chead * 3 + crole, lane, tme);
  if (ext) lds_wait(L + CNT + 36, 4);
}

DI u32 role_load(lp L, const char* x, const char* w, const char* wo_g, char* ws, bool isq, int rbi, int cg, int c0,
                  int lw, int lane) {
  __builtin_amdgcn_s_setprio(2);
  const char* xr = x + rbi * (isq ? 160 * XROW : 128 * XROW);
  const u32 xl = XRING + lw * 4096, XB = isq ? 20480 : 16384;
  u32 xo[5], wo[5];
  xo[0] = (32 * lw + (lane >> 3)) * XROW + (((lane & 7) ^ (lane >> 4)) << 4);
  xo[1] = (xo[0] ^ 64) + 8192; xo[2] = xo[0] + 16384; xo[3] = xo[1] + 16384;
  xo[4] = (xo[0] ^ ((lw & 1) * 64)) + (rbi < 12 ? (128 - 24 * lw) * XROW : 0);
  auto issx2 = [&](const char* xb, int slot) {
    glds4(xb, xl + slot * XB, xo[0], xo[1], xo[2], xo[3]);
    if (isq) glds(xb, xo[4], XRING + slot * XB + 16384 + lw * 1024);
  };
  auto issx = [&](int cidx, int slot) { issx2(xr + cidx * 128, slot); };
  auto issw = [&](int cidx, int slot) {
    const char* wb = w + cidx * (64 * WROW);
    const u32 lb = slot * 18432 + lw * 4608;
    glds4(wb, lb, wo[0], wo[1], wo[2], wo[3]);
    glds_lo(wb, wo[4], lb + 4096);
  };
  auto nxt = [](int c) { return c == 8 ? 0 : c + 1; };
  issx(c0, 0);
  SCHED();
  {
    const int cb0 = (isq ? 288 * cg : 1152 + 144 * cg) + 16 * lw * WROW, cb1 = isq ? cb0 : cb0 + 1008;
#pragma unroll
    for (int i = 0; i < 5; ++i) {
      const int f = i < 4 ? 64 * i + lane : 256 + (lane & 31);
      const int lr = (f * 3641) >> 16, cc = f - lr * 18;
      wo[i] = (lr ^ ((lr >> 1) & 4)) * WROW + (cc < 9 ? cb0 : cb1) + 16 * cc - (i < 4 ? 1024 * i : 0);
    }
  }
  issw(c0, 0);
  int c = nxt(c0);
  const int fr = lane & 15, fh = lane >> 4;
  const rsrc_t rs = wsrsrc(ws);
  issx(c, 1); issw(c, 1);
  c = nxt(c);
  const char* wb = w + c * (64 * WROW);
  const char* xb = xr + c * 128;
  asm volatile("" : "+s"(wb), "+s"(xb));
  if (isq) waitv<10>(); else waitv<9>();
  BAR();
  issx2(xb, 2);
#pragma unroll
  for (int k = 0; k < 7; ++k) {
    const u32 lb = ((k + 2) & 3) * 18432 + lw * 4608;
    glds4(wb, lb, wo[0], wo[1], wo[2], wo[3]);
    glds_lo(wb, wo[4], lb + 4096);
    if (k < 6) {
      c = nxt(c);
      xb = xr + c * 128; wb = w + c * (64 * WROW);
      asm volatile("" : "+s"(wb), "+s"(xb));
      issx2(xb, (k + 3) & 3);
      if (isq) waitv<15>(); else waitv<13>();
    } else {
      if (isq) waitv<10>(); else waitv<9>();
    }
    BAR();
  }
  if (lw == 1 && lane < 12) w32(L + CNT + lane * 4, 0);
  if (!isq && (rbi & 3) < 3) {
    const char* src = wo_g + (4 * cg + (rbi >> 2)) * (18 * XROW) + lw * 5184;
    glds4(src, WOI + lw * 5184, lane * 16, lane * 16, lane * 16, lane * 16);
    glds(src + 4096, lane * 16, WOI + lw * 5184 + 4096);
    if (lane < 4) glds(src + 5120, lane * 16, WOI + lw * 5184 + 5120);
    waitv<6>();
  } else {
    waitv<0>();
  }
  WAITL0();
  BAR();
  u32 tme = ld_tag(rs);
  __builtin_amdgcn_s_setprio(0);
  if (isq) {
    PIN(tme);
    if (lds_arrive(L + CNT + 44, lane) == 7) st_flag(rs, FL_PQ + rbi * 4 + cg, lane, tme);
    return tme;
  }
  if (lw == 0) w128(L + KVA + lane * 16, u32x4{0, 0, 0, 0});
  const bool isz = lane < 48;
  const u32 kb = WS_MBKV + (cg * 16 + 4 * lw) * MBKV_SZ + rbi * 640 + (lane & 7) * 16;
  u32 oa = isz ? (lane < 4 ? kb + 3 * MBKV_SZ : OOR) : kb + ((lane >> 3) & 1) * MBKV_SZ + 512;
  u32 ob = isz ? (lane < 4 ? kb + 3 * MBKV_SZ + 64 : OOR) : lane < 56 ? kb + 2 * MBKV_SZ + 256 : OOR;
  int sa = isz ? (lane & 3) * 64 : ((((lane >> 2) & 2) + ((lane >> 1) & 1)) * 16 + 8 + 2 * lw + (lane & 1)) * 4;
  int sb = isz ? (lane & 3) * 64 : ((lw >> 1) * 16 + 8 + 2 * lw + (lane & 1)) * 4;
  lp bk = L + XCH + lane * 16 + lw * 1024, bx = L + XCH + lane * 16 + 8192;
  const bool own = (fr >> 1) == 4 + lw;
  PIN(oa); PIN(ob); PIN(sa); PIN(sb); PIN(bk); PIN(bx); PIN(tme);
  waitv<0>();
  WAITL0();
  BAR();
  {
    u32x4 kf[4], xf[4];
#pragma unroll
    for (int r = 0; r < 4; ++r) { xf[r] = r128(bx + r * 9216); kf[r] = r128(bk + r * 9216); }
    SCHED();
    f32x4 p2 = {0.f, 0.f, 0.f, 0.f}, p4 = p2;
#pragma unroll
    for (int r = 0; r < 4; ++r) {
      u32x4 xo;
#pragma unroll
      for (int d = 0; d < 4; ++d) xo[d] = own ? xf[r][d] : 0x3f803f80u;
      p2 = mfma(kf[r], xo, p2); p4 = mfma(xf[r], xo, p4);
    }
    const u32 a0 = __builtin_amdgcn_ds_bpermute(sa, pk(p2[0], p2[1])), a1 = __builtin_amdgcn_ds_bpermute(sa, pk(p2[2], p2[3]));
    const u32 b0 = __builtin_amdgcn_ds_bpermute(sb, pk(p4[0], p4[1])), b1 = __builtin_amdgcn_ds_bpermute(sb, pk(p4[2], p4[3]));
    st_sc1(rs, oa, u32x4{a0, tme, a1, tme});
    st_sc1(rs, ob, u32x4{b0, tme, b1, tme});
  }
  kv_consume(L, rs, rbi, cg, tme, lw, lane);
  return tme;
}

DI u32 role_kv(lp L, char* ws, int rb, int g, int hi, int lane) {
  const int fr = lane & 15, fh = lane >> 4;
  const rsrc_t rs = wsrsrc(ws);
  const int c0 = g * 16 + 4 * hi + (lane >> 5), c1 = g * 16 + 4 * hi + 2;
  u32 d0 = WS_MBKV + c0 * MBKV_SZ + rb * 640 + (lane & 31) * 16;
  u32 d1 = fh == (hi >> 1) ? WS_MBKV + c1 * MBKV_SZ + rb * 640 + fr * 16 : OOR;
  lp xw = L + XCH + hi * 9216 + lane * 16, bk = L + XCH + hi * 1024 + lane * 16, bx = L + XCH + 8192 + lane * 16;
  PIN(d0); PIN(d1); PIN(xw); PIN(bk); PIN(bx);
  u32 tme;
  f32x4 acc[2][9];
  gemm_main<0>(L, hi, lane, acc, [&]() {
    tme = ld_tag(rs);
    const int n = rb * 4 + hi;
    auto piece = [&](int p) { const int cb = (p * 43) >> 9, r = p - cb * 12, ks = (r * 11) >> 5;
                              return WS_M + cb * 110592 + (4 * (g >> 1) + ks) * 6144 + (g & 1) * 3072 + (r - 3 * ks) * 1024; };
    const u32x4 ps = {POISON, POISON, POISON, POISON};
    __builtin_amdgcn_raw_buffer_store_b128(ps, rs, lane * 16, piece(n), 16);
    if (n < 20) {
      const int e = n - 8;
      __builtin_amdgcn_raw_buffer_store_b128(ps, rs, n < 8 || lane < 48 ? lane * 16 : OOR,
                                             n < 8 ? piece(64 + n) : WS_M + (e >> 1) * 110592 + 98304 + (2 * (g >> 1) + (e & 1)) * 1536 + (g & 1) * 768, 16);
    }
    SCHED();
  });
  {
#pragma unroll
    for (int t = 0; t < 4; ++t) {
      w128(xw + t * 1024, pack2(phi4(acc[0][t]), phi4(acc[1][t])));
      w128(xw + 4096 + t * 1024, pack2(acc[0][4 + t], acc[1][4 + t]));
    }
    const u32x4 xp = pack2(phi4(acc[0][8]), phi4(acc[1][8])), xr = pack2(acc[0][8], acc[1][8]);
    w128(xw + 8192, fr < 8 ? xp : xr);
  }
  PIN(tme);
  waitv<0>();
  WAITL0();
  BAR();
  {
    u32x4 xf[4], vf[4], kf[4];
#pragma unroll
    for (int r = 0; r < 4; ++r) { kf[r] = r128(bk + r * 9216); vf[r] = r128(bk + 4096 + r * 9216); }
    SCHED();
#pragma unroll
    for (int r = 0; r < 4; ++r) xf[r] = r128(bx + r * 9216);
    SCHED();
    f32x4 p1 = {0.f, 0.f, 0.f, 0.f}, p3 = p1;
#pragma unroll
    for (int r = 0; r < 4; ++r) p1 = mfma(kf[r], vf[r], p1);
#pragma unroll
    for (int r = 0; r < 4; ++r) p3 = mfma(xf[r], vf[r], p3);
    st_sc1(rs, d0, u32x4{pk(p1[0], p1[1]), tme, pk(p1[2], p1[3]), tme});
    st_sc1(rs, d1, u32x4{pk(p3[0], p3[1]), tme, pk(p3[2], p3[3]), tme});
  }
  return tme;
}

DI u32 role_q(lp L, char* ws, int jq, int hg, int w, int lane) {
  const rsrc_t rs = wsrsrc(ws), rq = __builtin_amdgcn_make_buffer_rsrc(ws, 0, WS_M, 0x00020000);
  const int ta = jq * 10 + (w >> 1) * 5 + (w & 1) * 2, tb = jq * 10 + (w >> 1) * 5 + 3 - (w & 1) * 3;
  u32 pa = WS_PQ + ta * (18 * 1024) + hg * 4096 + (w & 1) * 2048 + lane * 16, pb = pa + (tb - ta) * (18 * 1024);
  u32 pe = WS_PQ + (ta + (lane >> 5)) * (18 * 1024) + 16384 + hg * 512 + (lane & 31) * 16;
  PIN(pa); PIN(pb); PIN(pe);
  f32x4 acc[5][5];
  u32 tme;
  gemm_main<1>(L, w, lane, acc, [&]() { tme = ld_tag(rs); });
#pragma unroll
  for (int n = 0; n < 10; ++n) {
    const int k = n / 5, i = n % 5;
    st_sc1(rq, (i < 3 ? pa + i * (18 * 1024) : pb + (i - 3) * (18 * 1024)) + k * 1024, pack2(phi4(acc[i][2 * k]), phi4(acc[i][2 * k + 1])));
    SCHED();
  }
  u32x2 ex[3];
#pragma unroll
  for (int i = 0; i < 3; ++i) { const f32x4 e = phi4(acc[i][4]); ex[i] = u32x2{pk(e[0], e[1]), pk(e[2], e[3])}; }
#pragma unroll
  for (int k = 0; k < 2; ++k) {
    const int ib = k ? 2 : 1;
    auto sx = __builtin_amdgcn_permlane32_swap(ex[2 * k][0], ex[ib][0], false, false);
    auto sy = __builtin_amdgcn_permlane32_swap(ex[2 * k][1], ex[ib][1], false, false);
    st_sc1(rq, k == 0 ? pe : lane < 32 && (w & 1) ? pe + 36 * 1024 : OOR, u32x4{sx[0], sy[0], sx[1], sy[1]});
  }
  PIN(tme);
  waitv<0>();
  if (lds_arrive(L + CNT + 44, lane) == 7) st_flag(rs, FL_PQ + jq * 4 + hg, lane, tme);
  return tme;
}

struct cx2 { f32x2 r, i; };
DI cx2 cmul(cx2 a, cx2 b) { return cx2{a.r * b.r - a.i * b.i, a.r * b.i + a.i * b.r}; }
DI cx2 csq(cx2 a) { return cx2{a.r * a.r - a.i * a.i, (a.r + a.r) * a.i}; }
DI cx2 cpow(f32x2 lr, f32x2 rv, float n) {
  cx2 o;
#pragma unroll
  for (int m = 0; m < 2; ++m) {
    float mag = __builtin_amdgcn_exp2f(lr[m] * n), ph = __builtin_amdgcn_fractf(rv[m] * n);
    o.r[m] = mag * __builtin_amdgcn_cosf(ph);
    o.i[m] = mag * __builtin_amdgcn_sinf(ph);
  }
  return o;
}
constexpr int UB = 4608, PIT = 544, STG = UB, TBL = STG + 16 * PIT, WVB = TBL + 4368;
constexpr int TPE = TBL, TPD = 1216, TPO = TBL + TPD, F2 = TBL + 2368, F2S = 144, F3 = F2 + 4 * F2S, F3S = 176, FCP = 960;
constexpr int FLG = CNT + 16;
constexpr int wvoff(int w) { return w * WVB; }
static_assert(8 * WVB <= GWL && WVB % 16 == 0, "lds");
struct SsmTab { u32 v[8][64][4]; };
constexpr SsmTab make_tab() {
  SsmTab t{};
  for (int l = 0; l < 64; ++l) {
    const int fr = l & 15, fh = l >> 4;
    const int lo[5] = {3, 6, 5, 4, 3}, hi[5] = {5, 10, 11, 12, 4};
    for (int k = 0; k < 5; ++k)
      for (int jj = 0; jj < 8; ++jj) {
        const int xx = fr + 16 - 8 * fh - jj;
        if (xx >= lo[k] && xx <= hi[k]) t.v[k][l][jj >> 1] |= (k == 4 ? 0xffffu : 0x3f80u) << (16 * (jj & 1));
      }
    for (int j = 0; j < 4; ++j)
      t.v[5][l][j] = fh < 2 ? F2 + (2 * (fr & 1) + (j & 1)) * F2S + (47 - 16 * fh - 8 * (j >> 1) - (fr >> 1)) * 2
                            : F3 + (fr & 1) * F3S + (63 - 32 * (fh - 2) - 8 * j - (fr >> 1)) * 2;
    t.v[6][l][0] = TBL + 1080 - 2 * (128 * (fh >> 1) + 4 * (fh & 1) + 8 * fr);
    t.v[6][l][1] = l < 32 ? TBL + 558 - 16 * l : STG + 14 + 16 * l;
    t.v[6][l][2] = l == 31 ? TBL + 60 : l == 63 ? TBL + 574 : l == 32 ? TBL + 1088 : STG + 4 * l;
    t.v[6][l][3] = l < 32 ? l * 16 : l < 36 ? TPE + (l - 32) * 16 : l < 40 ? TPE + 1088 + (l - 36) * 16
                   : l < 44 ? TPO + (l - 40) * 16 : l < 48 ? TPO + 1088 + (l - 44) * 16 : F2;
    t.v[7][l][0] = fr * 32 + fh * 16;
    t.v[7][l][1] = ((fr & 1) ? TPO + 2 : TPE) + (16 - fr + 8 * fh) * 2;
    t.v[7][l][2] = F2 + ((fr & 1) ? 0 : FCP + 2) + (15 - fr + 4 * fh) * 2;
    t.v[7][l][3] = STG + fr * PIT + fh * 16;
  }
  return t;
}
__device__ const SsmTab ssm_tab = make_tab();

DI u32 role_ssm(lp L, const char* x, const u32* logA, const u32* Aim, const u32* Cre, const u32* Cim, const u16* logdt,
                 const u16* Dsk, char* ws, int cb, int wave, int lane) {
  const int chn = cb * 8 + wave;
  const GAS u32x4* tab = (const GAS u32x4*)ssm_tab.v[2] + lane;
  PIN(tab);
  u32 pa, pi, pcr, pci, pdt, pds;
  u32x4 o5, o6, o7;
  asm volatile("s_nop 4\n\tglobal_load_dword %0, %9, %12\n\tglobal_load_dword %1, %9, %13\n\tglobal_load_ushort %4, %10, %16\n\t"
               "global_load_dword %2, %9, %14\n\tglobal_load_dword %3, %9, %15\n\tglobal_load_ushort %5, %10, %17\n\t"
               "global_load_dwordx4 %6, %11, %18\n\tglobal_load_dwordx4 %7, %11, %18 offset:1024\n\tglobal_load_dwordx4 %8, %11, %18 offset:2048"
               : "=&v"(pa), "=&v"(pi), "=&v"(pcr), "=&v"(pci), "=&v"(pdt), "=&v"(pds), "=&v"(o5), "=&v"(o6), "=&v"(o7)
               : "v"((chn * 64 + lane) * 4), "v"(chn * 2), "v"(lane * 16), "s"(logA), "s"(Aim), "s"(Cre), "s"(Cim), "s"(logdt), "s"(Dsk),
                 "s"(ssm_tab.v[5])
               : "memory");
  lp wv = L + wave * WVB;
  const int tid = wave * 64 + lane;
  w128(wv + F2 + lane * 16, u32x4{0, 0, 0, 0});
  if (lane < 56) w128(wv + F2 + 1024 + lane * 16, u32x4{0, 0, 0, 0});
  u32x4 ur[4];
  const char* xc = x + cb * 16;
  const u32 uo = 4 * tid * XROW;
  auto ldu = [&](int i) { SCHED(); ur[i] = *(const u32x4*)(xc + uo + i * XROW); SCHED(); };
  if (tid < 12) w32(L + CNT + tid * 4, 0);
  WAITL0();
  BAR();
  ldu(0);
  waitv<1>();
  asm volatile("" : "+v"(pa), "+v"(pi), "+v"(pcr), "+v"(pci), "+v"(pdt), "+v"(pds), "+v"(o5), "+v"(o6), "+v"(o7));
  const float ldt = bf2f(pdt), dsk = bf2f(pds);
  w128(wv + o6[3], u32x4{0, 0, 0, 0});
  lp fw[4] = {wv + o5[0], wv + o5[1], wv + o5[2], wv + o5[3]};
  lp e0 = wv + o6[0], e1 = wv + o6[1], e2 = wv + o6[2];
  lp ua = wv + o7[0], ta = wv + o7[1], t2 = wv + o7[2], fa = wv + o7[3], pw = wv + STG + lane * 8;
  CFENCE();
  const float dt = rbf(__expf(ldt));
  f32x2 lr = {-rbf(__expf(bf2f(pa))) * dt, -rbf(__expf(bfhi(pa))) * dt};
  f32x2 rv = {bf2f(pi) * dt, bfhi(pi) * dt};
  lr *= L2E; rv *= 0.15915494309f;
  const cx2 w1 = cpow(lr, rv, 1.f), wb = cpow(lr, rv, 128.f);
  cx2 w8 = csq(csq(csq(w1)));
  w8.i = -w8.i;
  const cx2 cc = {f32x2{bf2f(pcr), bfhi(pcr)} * (2.f * dt), f32x2{bf2f(pci), bfhi(pci)} * (2.f * dt)};
  ldu(1);
  auto put = [&](int row, cx2 v) { w64(pw + row * PIT, u32x2{pk(v.r[0], v.i[0]), pk(v.r[1], v.i[1])}); };
  auto get = [&](u32x4 (&f)[8]) {
    CFENCE();
#pragma unroll
    for (int s = 0; s < 8; ++s) f[s] = r128(fa + s * 64);
    CFENCE();
    SCHED();
  };
  const f32x4 zero = {0.f, 0.f, 0.f, 0.f};
  u32x4 yf[8], xf[8];
  {
    cx2 y = {f32x2{1.f, 1.f}, f32x2{0.f, 0.f}};
    put(0, y); y = w8; put(1, y);
#pragma unroll
    for (int r = 2; r < 16; ++r) { y = cmul(y, w8); put(r, y); if (r == 7) ldu(2); }
    get(yf);
    ldu(3);
  }
  cx2 e[4];
  {
    cx2 v = cc, u8 = cmul(cc, wb);
#pragma unroll
    for (int a = 0; a < 8; ++a) {
      v = cmul(v, w1); u8 = cmul(u8, w1);
      put(a, v); put(8 + a, u8);
      if (a & 1) e[a >> 1] = u8;
    }
    get(xf);
  }
  u32x4 xg[8], xh[8];
#pragma unroll
  for (int n = 0; n < 8; ++n) { e[n & 3] = cmul(e[n & 3], wb); put(n, e[n & 3]); }
  put(8, cc);
  get(xg);
#pragma unroll
  for (int b = 0; b < 4; ++b) {
    e[1] = cmul(e[1], wb); e[3] = cmul(e[3], wb);
    put(2 * b, e[1]); put(2 * b + 1, e[3]);
  }
#pragma unroll
  for (int n = 0; n < 8; ++n) { e[3] = cmul(e[3], wb); put(8 + n, e[3]); }
  get(xh);
#pragma unroll
  for (int c = 0; c < 4; ++c) {
    w64(L + wvoff(2 * c) + 512 + tid * 8, u32x2{__builtin_amdgcn_perm(ur[1][c], ur[0][c], 0x05040100), __builtin_amdgcn_perm(ur[3][c], ur[2][c], 0x05040100)});
    w64(L + wvoff(2 * c + 1) + 512 + tid * 8, u32x2{__builtin_amdgcn_perm(ur[1][c], ur[0][c], 0x07060302), __builtin_amdgcn_perm(ur[3][c], ur[2][c], 0x07060302)});
  }
  CFENCE();
  if (lane == 0) __hip_atomic_fetch_add((LAS u32*)(L + FLG), 1u, __ATOMIC_RELAXED, __HIP_MEMORY_SCOPE_WORKGROUP);
  CFENCE();
  SCHED();
  f32x4 k0 = zero, k1 = zero, k2 = zero;
#pragma unroll
  for (int n = 0; n < 8; ++n) { k0 = mfma(xf[n], yf[n], k0); k1 = mfma(xg[n], yf[n], k1); }
  SCHED();
#pragma unroll
  for (int n = 0; n < 8; ++n) k2 = mfma(xh[n], yf[n], k2);
  const u32 d32 = pk(k0[3], k0[2]);
  {
    const u32 d10 = pk(k0[1], k0[0]), d21 = pk(k0[2], k0[1]);
    w64(e0, u32x2{d32, d10});
    w16(e0 + TPD + 2, d32); CFENCE(); w32(e0 + TPD + 4, d21); CFENCE(); w16(e0 + TPD + 8, d10 >> 16);
  }
  {
    const u32 c33 = pk(k1[3], k1[3]), c32 = pk(k1[3], k1[2]), c11 = pk(k1[1], k1[1]), c10 = pk(k1[1], k1[0]), c21 = pk(k1[2], k1[1]);
    w128(e1 + TPD + 2, u32x4{c33, c32, c11, c10});
    w16(e1, c33); CFENCE(); w32(e1 + 2, c33); CFENCE(); w32(e1 + 6, c21); CFENCE(); w32(e1 + 10, c11); CFENCE(); w16(e1 + 14, c10 >> 16);
    u32 v = pk(k1[0] + dsk, 0.f);
    PIN(v);
    v = lane < 32 ? c33 : lane < 48 ? v : d32;
    w16(e2, v); w16(e2 + TPD + 2, v);
  }
  const rsrc_t rs = wsrsrc(ws);
  u32 tme = ld_tag(rs);
  u32x4 BX[5];
#pragma unroll
  for (int k = 0; k < 5; ++k) BX[k] = tab[(k - 2) * 64];
  const u32 bo = (lane & 16) * 32 + (lane & 12) * 32 + (lane & 32) * 2 + (lane & 3) * 16;
  {
    const u32 f01 = pk(k2[0], k2[1]), f23 = pk(k2[2], k2[3]);
#pragma unroll
    for (int j = 0; j < 4; ++j) { const u32 v = (j < 2 ? f01 : f23) >> (16 * (j & 1)); w16(fw[j], v); w16(fw[j] + FCP + 2, v); }
  }
  const u32 k1024 = __builtin_amdgcn_readlane(pk(k2[3], k2[3]), 31);
  CFENCE();
  u32x4 T[10];
#pragma unroll
  for (int n = 0; n < 20; ++n) {
    const int k = n >> 1, hf = n & 1;
    const int o = k < 3 ? hf * F2S + 64 - 32 * k : k < 6 ? (2 + hf) * F2S + 64 - 32 * (k - 3) : 4 * F2S + hf * F3S + 96 - 32 * (k - 6);
    T[k][2 * hf] = r32(t2 + o); T[k][2 * hf + 1] = r32(t2 + o + 4);
  }
  const u32x4 F32 = r4x32(ta);
  for (u32 spin = 0; spin < (1u << 20); ++spin) {
    CFENCE();
    if (__builtin_amdgcn_readfirstlane(r32(L + FLG)) == 8) break;
    __builtin_amdgcn_s_sleep(1);
  }
  CFENCE();
  u32x4 U[2][8], Fa[2], Fb[2];
  auto rdn = [&](int d0, int n) {
    const int q = d0 & 1;
    if (n == 0) { Fa[q][0] = r32(ta + 1024 - d0 * 64); Fa[q][1] = r32(ta + 1028 - d0 * 64); }
    else if (n == 1) { Fa[q][2] = r32(ta + 1032 - d0 * 64); Fa[q][3] = r32(ta + 1036 - d0 * 64); }
    else if (n < 10) U[q][n - 2] = r128(ua + 480 + (n - 2) * 512 - d0 * 64);
    else if (n == 10) { Fb[q][0] = r32(ta + 512 - d0 * 64); Fb[q][1] = r32(ta + 516 - d0 * 64); }
    else { Fb[q][2] = r32(ta + 520 - d0 * 64); Fb[q][3] = r32(ta + 524 - d0 * 64); }
  };
#pragma unroll
  for (int n = 0; n < 12; ++n) rdn(7, n);
  SCHED();
  u32x4 W1[6];
  f32x4 acc[8];
#pragma unroll
  for (int d0 = 7; d0 >= 0; --d0) {
    const int q = d0 & 1;
#pragma unroll
    for (int m = 0; m < 15; ++m) {
      if (d0 < 7 && m == 0) __builtin_amdgcn_s_waitcnt(0xC67F);
      if (d0 < 7 && m == 4) __builtin_amdgcn_s_waitcnt(0xC47F);
      if (m >= 8) acc[m - 7] = mfma(Fb[q], U[q][m - 8], acc[m - 7]);
      else acc[m] = mfma(Fa[q], U[q][m], d0 == 7 ? zero : acc[m]);
      SCHED();
      if (d0 > 0) { if (m < 12) rdn(d0 - 1, m); }
      else if (m < 6) W1[m] = r128(ua + 464 + m * 512);
      SCHED();
    }
  }
  {
    const u32x4 FX = BX[4] & k1024;
    f32x4 s[6];
    u32x4 SA, SB, SC;
    auto box = [&](int b) {
      s[0] = mfma(U[0][b], BX[0], zero); s[1] = mfma(U[0][b], BX[1], zero);
      s[2] = mfma(W1[b], BX[0], zero); s[3] = mfma(W1[b], BX[1], zero);
      if (b < 4) { s[4] = mfma(U[0][b], BX[2], zero); s[5] = mfma(W1[b], BX[3], zero); }
    };
    box(0);
#pragma unroll
    for (int b = 0; b < 6; ++b) {
      acc[b + 2] = mfma(F32, U[0][b], acc[b + 2]);
      if (b < 4) acc[b + 4] = mfma(FX, U[0][b], acc[b + 4]);
      SA = pack2(s[0], s[1]); SB = pack2(s[2], s[3]);
      if (b < 4) SC = pack2(s[4], s[5]);
      if (b < 5) box(b + 1);
#pragma unroll
      for (int d = 0; d < 3; ++d)
        if (b + 2 + d < 8) acc[b + 2 + d] = mfma(SA, T[d], acc[b + 2 + d]);
#pragma unroll
      for (int d = 0; d < 3; ++d)
        if (b + 2 + d < 8) acc[b + 2 + d] = mfma(SB, T[3 + d], acc[b + 2 + d]);
      if (b < 4) {
#pragma unroll
        for (int d = 0; d < 4; ++d)
          if (b + 4 + d < 8) acc[b + 4 + d] = mfma(SC, T[6 + d], acc[b + 4 + d]);
      }
    }
#pragma unroll
    for (int i = 0; i < 76; ++i) { __builtin_amdgcn_sched_group_barrier(0x8, 1, 0); __builtin_amdgcn_sched_group_barrier(0x2, 1, 0); }
  }
  const u32 dst = WS_BT + chn * BTP + bo;
#pragma unroll
  for (int i = 0; i < 8; i += 2) {
    auto sx = __builtin_amdgcn_permlane16_swap(pk(acc[i][0], acc[i][1]), pk(acc[i + 1][0], acc[i + 1][1]), false, false);
    auto sy = __builtin_amdgcn_permlane16_swap(pk(acc[i][2], acc[i][3]), pk(acc[i + 1][2], acc[i + 1][3]), false, false);
    st_sc1(rs, dst + i * 512, u32x4{sx[0], sy[0], sx[1], sy[1]});
  }
  PIN(tme);
  lds_arrive(L + CNT + 4, lane);
  return tme;
}

constexpr int PB_B = WOI - 12288;
DI float dot2(u32 a, u32 b, float c) { return __builtin_amdgcn_fdot2_f32_bf16(__builtin_bit_cast(bf16x2, a), __builtin_bit_cast(bf16x2, b), c, false); }
DI void scale4(u32x4& v, u32 rl, u32 rh) {
  u32 x0 = v[0], x1 = v[1], x2 = v[2], x3 = v[3];
  float t0, t1, t2, t3, t4, t5, t6, t7;
  asm("v_dot2_f32_bf16 %4, %0, %12, 0\n\tv_dot2_f32_bf16 %5, %0, %13, 0\n\tv_dot2_f32_bf16 %6, %1, %12, 0\n\tv_dot2_f32_bf16 %7, %1, %13, 0\n\t"
      "v_dot2_f32_bf16 %8, %2, %12, 0\n\tv_dot2_f32_bf16 %9, %2, %13, 0\n\tv_dot2_f32_bf16 %10, %3, %12, 0\n\tv_dot2_f32_bf16 %11, %3, %13, 0\n\t"
      "v_cvt_pk_bf16_f32 %0, %4, %5\n\tv_cvt_pk_bf16_f32 %1, %6, %7\n\tv_cvt_pk_bf16_f32 %2, %8, %9\n\tv_cvt_pk_bf16_f32 %3, %10, %11"
      : "+v"(x0), "+v"(x1), "+v"(x2), "+v"(x3), "=&v"(t0), "=&v"(t1), "=&v"(t2), "=&v"(t3), "=&v"(t4), "=&v"(t5), "=&v"(t6), "=&v"(t7)
      : "v"(rl), "v"(rh));
  v = u32x4{x0, x1, x2, x3};
}
DI void phase_b(lp L, char* ws, char* out, int id, u32 tag, int btf, bool early, int wave, int lane) {
  const int rb = (id * 171) >> 10, cb = id - rb * 6;
  const rsrc_t rs = wsrsrc(ws);
  auto btdone = [&]() {
    if (btf) { waitv<0>(); if (lds_arrive(L + FLG + 4, lane) == 7) st_flag(rs, btf, lane, tag); }
  };
  if (wave < 4) {
    const u32 vm = WS_M + cb * 110592 + wave * 4608 + lane * 16;
    auto issue = [&](int gq) { glds45s(ws + gq * 18432, gq * 18432 + wave * 4608, vm); };
    u32 bo[3];
#pragma unroll
    for (int i = 0; i < 3; ++i) {
      const int ir = wave * 24 + i * 8 + (lane >> 3);
      bo[i] = WS_BT + (cb * 96 + ir) * BTP + rb * 128 + (((lane & 7) ^ (((ir * 43) >> 8) & 7)) << 4) - 1024 * i;
      PIN(bo[i]);
    }
    u32 fo = lane < 3 ? WS_FL + (FL_BT + cb * 12 + wave * 3 + lane) * FLS : lane == 3 && wave == 0 ? WS_FL + (FL_G + (rb >> 3)) * FLS : OOR;
    PIN(fo);
    btdone();
    lds_wait(L + CNT + 12, 4);
    __builtin_amdgcn_s_sleep(M_DELAY);
    __builtin_amdgcn_s_setprio(2);
    auto issb = [&]() {
      asm volatile("s_mov_b32 m0, %4\n\ts_nop 3\n\tglobal_load_lds_dwordx4 %0, %3 sc1\n\tglobal_load_lds_dwordx4 %1, %3 offset:1024 sc1\n\t"
                   "global_load_lds_dwordx4 %2, %3 offset:2048 sc1"
                   ::"v"(bo[0]), "v"(bo[1]), "v"(bo[2]), "s"(ws), "s"(PB_B + wave * 3072) : "memory");
      if (wave == 0)
        asm volatile("s_mov_b32 m0, %2\n\ts_nop 3\n\tglobal_load_lds_dword %0, %1 sc1" ::"v"(WS_G + rb * 256 + lane * 4), "s"(ws), "s"(PB_B - 256) : "memory");
    };
#pragma unroll 1
    for (int pass = 0; pass < 2; ++pass) {
      u32 fv;
      asm volatile("s_nop 4\n\tbuffer_load_dword %0, %1, %2, 0 offen sc1" : "=&v"(fv) : "v"(fo), "s"(rs) : "memory");
      issue(0); issue(1); issue(2); issue(3); issue(4); issue(5);
      asm volatile("s_waitcnt vmcnt(20)" : "+v"(fv)::"memory");
      fo = fv == tag ? OOR : fo;
      const bool bok = __all(fo == OOR);
      BAR();
      if (bok) { issb(); waitv<13>(); } else waitv<10>();
      BAR();
      if (!bok) { wait_flags<2>(rs, fo, tag); issb(); }
      waitv<3>();
      BAR();
      waitv<0>(); BAR();
      BAR();
      if (pass || !__builtin_amdgcn_readfirstlane(r32(L + CNT + 24))) break;
      wait_flags<4>(rs, lane < 24 ? WS_FL + (FL_M + wave * 24 + lane) * FLS : OOR, tag);
      lds_meet4(L + CNT + 8, lane);
    }
    return;
  }
  const int cw = wave - 4, fr = lane & 15, fh = lane >> 4;
  const int zh = lane >> 3, zi = lane & 7;
  lp zw = L + GWL + (zi < 4 ? cw * 256 + (zh & 1) * 128 + (zi >> 1) * 64 + (zh >> 1) * 16 + (zi & 1) * 8
                     : zi == 4 + ((zh >> 1) & 1) && !(zh & 1) ? 1024 + (cw >> 1) * 64 + (zh >> 1) * 16 + (cw & 1) * 8 : 1216);
  lp zp = L + GWL + fh * 16;
  lp B0 = L + fh * 1536 + fr * 96 + (fr >> 3) * 16, B1 = L + fh * 1536 + fr * 96 + 16 - (fr >> 3) * 16;
  lp B0h = B0 + 49152, B1h = B1 + 49152, Be0 = L + 98304 + fh * 384 + fr * 24 + (fh & 1) * 4, Be1 = Be0 + 4 - (fh & 1) * 8;
  lp Bb = L + PB_B + fr * 768 + (((cw + 4 * (fh >> 1)) ^ (fr & 7)) << 4) + (fh & 1) * 8;
  u32 dst = (rb * 64 + cw * 16 + fh * 4) * XROW + cb * 192 + fr * 12;
  u32 l16 = lane * 16, pe = (lane & 31) * 16 + (lane >> 5) * 8;
  PIN(zw); PIN(zp); PIN(B0); PIN(B1); PIN(B0h); PIN(B1h); PIN(Be0); PIN(Be1); PIN(Bb); PIN(dst); PIN(l16); PIN(pe);
  btdone();
  if (cw == 0) {
    if (early) __builtin_amdgcn_s_sleep(PQ_DELAY);
    const int jq = ((4 * rb + (lane >> 2) * 3) * 205) >> 11;
    wait_flags<PQ_SLEEP>(rs, lane < 8 ? WS_FL + (FL_PQ + jq * 4 + (lane & 3)) * FLS : OOR, tag);
    lds_arrive(L + CNT + 4, lane);
  }
  lds_wait(L + CNT + 4, btf ? 9 : 1);
  SCHED();
  u32x4 a[18];
  const u32 pq = WS_PQ + (4 * rb + cw) * 18432;
#pragma unroll
  for (int s = 16; s < 18; ++s) {
    const u32x2 lo = __builtin_amdgcn_raw_buffer_load_b64(rs, pe + (s - 16) * 1024, pq + 16384, 16);
    const u32x2 hi = __builtin_amdgcn_raw_buffer_load_b64(rs, pe + (s - 16) * 1024 + 512, pq + 16384, 16);
    a[s] = u32x4{lo[0], lo[1], hi[0], hi[1]};
  }
  auto lda = [&](int s) { a[s] = __builtin_amdgcn_raw_buffer_load_b128(rs, l16 + (s & 3) * 1024, pq + (s >> 2) * 4096, 16); };
#pragma unroll
  for (int s = 0; s < ZPOS; ++s) lda(s);
  SCHED();
  {
    u32x4 zm = __builtin_amdgcn_raw_buffer_load_b128(rs, l16, WS_ZC + cw * 1024, 16);
    SCHED();
#pragma unroll
    for (int s = ZPOS; s < 16; ++s) lda(s);
    SCHED();
    if (!__all(zm[1] == tag && zm[3] == tag)) {
      for (u32 spin = 0; spin < SPIN_CAP; ++spin) {
        CFENCE();
        zm = __builtin_amdgcn_raw_buffer_load_b128(rs, l16, WS_ZC + cw * 1024, 16);
        if (__all(zm[1] == tag && zm[3] == tag)) break;
        __builtin_amdgcn_s_sleep(POLL_SLEEP);
      }
    }
    w64(zw, u32x2{zm[0], zm[2]});
    lds_arrive(L + CNT + 12, lane);
  }
  lds_wait(L + CNT + 12, 4);
  u32x4 z[18];
#pragma unroll
  for (int s = 0; s < 18; ++s) z[(s + 16) % 18] = r128(zp + ((s + 16) % 18) * 64);
  SCHED();
#pragma unroll
  for (int v = 0; v < 8; ++v) {
    const int u = v;
    const u32 xe = a[16 + (u >> 2)][u & 3], zeu = z[16 + (u >> 2)][u & 3];
    float d0 = dot2(xe, zeu, EPS), d1 = 0.f;
#pragma unroll
    for (int d = 0; d < 4; ++d) {
      const u32 p = a[2 * u][d], q = a[2 * u + 1][d], y = z[2 * u][d], w_ = z[2 * u + 1][d];
      d0 = dot2(p, y, d0); d1 = dot2(q, w_, d1);
    }
    const u32 rh = pk(0.f, __builtin_amdgcn_rcpf(d0 + d1)), rl = rh >> 16;
    scale4(a[2 * u], rl, rh); scale4(a[2 * u + 1], rl, rh);
    a[16 + (u >> 2)][u & 3] = pk(bf2f(xe) * __uint_as_float(rh), bfhi(xe) * __uint_as_float(rh));
  }
#pragma unroll
  for (int s = 0; s < 18; ++s) PIN(a[s]);
  f32x4 acc[6], bf[6], g;
  u32x4 b[3][6];
  u32x2 bv[6];
  auto rd = [&](int n, int t) {
    if (n >= 18) return;
    if (n >= 16) {
      lp e = (t & 1 ? Be1 : Be0) + (n - 16) * 6144 + (t >> 1) * 8;
      b[n % 3][t] = u32x4{r32(e), r32(e + 1536), r32(e + 3072), r32(e + 4608)};
    } else b[n % 3][t] = r128((n < 8 ? (t & 1 ? B1 : B0) : (t & 1 ? B1h : B0h)) + (n & 7) * 6144 + (t >> 1) * 32);
    SCHED();
  };
  const rsrc_t ro = __builtin_amdgcn_make_buffer_rsrc(out, 0, SEQ * XROW, 0x00020000);
#pragma unroll 1
  for (int pass = 0; pass < 2; ++pass) {
    BAR();
#pragma unroll
    for (int t = 0; t < 12; ++t) rd(t / 6, t % 6);
#pragma unroll
    for (int n = 0; n < 18; ++n) {
      if (n == 4 || n == 10 || n == BRD) BAR();
      if (n == BRD) { g = __builtin_bit_cast(f32x4, r128(L + PB_B - 256 + cw * 64 + fh * 16)); SCHED(); }
#pragma unroll
      for (int t = 0; t < 6; ++t) {
        if (n > 0 && n < BRD && t % 3 == 0) __builtin_amdgcn_s_waitcnt(0xC97F);
        acc[t] = mfma(a[n], b[n % 3][t], n ? acc[t] : f32x4{0.f, 0.f, 0.f, 0.f});
        SCHED();
        rd(n + 2, t);
        if (n == BRD) { bv[t] = r64(Bb + t * 128); SCHED(); }
        if (n >= 16) {
          const int h = n - 16;
          float lo = bf2f(bv[t][h]), hi = bfhi(bv[t][h]);
          lo = __builtin_fmaf(-g[2 * h], lo, lo); PIN(lo);
          hi = __builtin_fmaf(-g[2 * h + 1], hi, hi); PIN(hi);
          bf[t][2 * h] = lo; bf[t][2 * h + 1] = hi;
          SCHED();
        }
      }
    }
    const float chk = acc[0][0] + acc[1][0] + acc[2][0] + acc[3][0] + acc[4][0] + acc[5][0];
    if (pass || !__any(chk != chk)) {
#pragma unroll
      for (int t = 0; t < 6; ++t) {
        f32x4 o = acc[t] * g + bf[t];
        PIN(o); acc[t] = o;
      }
      typedef __attribute__((ext_vector_type(3))) unsigned u32x3;
#pragma unroll
      for (int j = 0; j < 4; ++j)
        __builtin_amdgcn_raw_buffer_store_b96(u32x3{pk(acc[0][j], acc[1][j]), pk(acc[2][j], acc[3][j]), pk(acc[4][j], acc[5][j])}, ro, dst + j * XROW, 0, AUX_OUT);
    } else {
      lds_arrive(L + CNT + 24, lane);
      WAITL0();
    }
    BAR();
    if (pass || !__builtin_amdgcn_readfirstlane(r32(L + CNT + 24))) break;
  }
}

DI u32 role_helper(lp L, const char* x, const char* gw, char* ws, const char* out, int Q, int wave, int lane) {
  {
    const rsrc_t rs = wsrsrc(ws);
    u32 tme = ld_tag(rs);
    u32x4 gv[9];
#pragma unroll
    for (int i = 0; i < 9; ++i) gv[i] = *(const u32x4*)(gw + i * 128 + (lane & 7) * 16);
    float res = 0.f;
#pragma unroll 4
    for (int p = 0; p < 8; ++p) {
      const char* xp = x + (u32)(Q * 512 + wave * 64 + (lane >> 3) * 8 + p) * XROW + (lane & 7) * 16;
      u32x4 xv[9];
#pragma unroll
      for (int i = 0; i < 9; ++i) xv[i] = *(const u32x4*)(xp + i * 128);
      float a = 0.f;
#pragma unroll
      for (int i = 0; i < 9; ++i)
#pragma unroll
        for (int d = 0; d < 4; ++d) {
          const u32 xa = xv[i][d], ga = gv[i][d];
          a = __builtin_amdgcn_fdot2_f32_bf16(__builtin_bit_cast(bf16x2, xa), __builtin_bit_cast(bf16x2, ga), a, false);
        }
      a += __builtin_bit_cast(float, __builtin_amdgcn_update_dpp(0, __builtin_bit_cast(int, a), 0xb1, 0xf, 0xf, false));
      a += __builtin_bit_cast(float, __builtin_amdgcn_update_dpp(0, __builtin_bit_cast(int, a), 0x4e, 0xf, 0xf, false));
      a += __builtin_bit_cast(float, __builtin_amdgcn_update_dpp(0, __builtin_bit_cast(int, a), 0x141, 0xf, 0xf, false));
      res = (lane & 7) == p ? a : res;
    }
    res = rbf(__builtin_amdgcn_rcpf(1.f + __builtin_amdgcn_exp2f(-L2E * rbf(res))));
    st32_sc1(rs, WS_G + (Q * 512 + wave * 64 + lane) * 4, __float_as_uint(res));
    PIN(tme);
    if (wave == 1 && lane < 12) w32(L + CNT + lane * 4, 0);
    waitv<0>();
    WAITL0();
    BAR();
    if (wave == 0) st_flag(rs, FL_G + Q, lane, tme);
    return tme;
  }
}

extern "C" __global__ __launch_bounds__(512, 2) void gm_k1(const char* x, const char* w, const char* wo, const u32* logA, const u32* Aim,
                                             const u32* Cre, const u32* Cim, const u16* logdt, const u16* Dsk, const char* gw, char* ws, char* out) {
  extern __shared__ __attribute__((aligned(16))) char smem[];
  lp L = (lp)smem;
  const int xcd = blockIdx.x & 7, l = blockIdx.x >> 3, loc = l - 7;
  const int wave = __builtin_amdgcn_readfirstlane(threadIdx.x >> 6), lane = threadIdx.x & 63;
  if (l < 2 && wave >= 4) {
    const char* pc; u32 len;
    asm volatile("s_getpc_b64 %0\n1:\n\ts_mov_b32 %1, gm_code_end-1b" : "=s"(pc), "=s"(len));
    touch(pc, min((u32)(((wave - 4) * 2 + l) * 64 + lane) * 128, len));
  }
  const bool isq = l < 7;
  const int Q = xcd >> 1;
  const int rq = l == 6 ? 6 : ((0xa730 >> (4 * Q)) & 15) + (l >> 1), rk = Q * 4 + (loc >> 2);
  const int cq = l == 6 ? Q : (xcd & 1) * 2 + (l & 1), ck = (xcd & 1) * 4 + (loc & 3);
  const int sq = 3 * (l >> 1) + 4 * (l & 1) + 1, sk = 2 * ((loc >> 2) + (loc & 3));
  int rbi = isq ? rq : rk, cg = isq ? cq : ck, c0 = isq ? sq : sk;
  c0 = c0 >= 9 ? c0 - 9 : c0;
  asm volatile("" : "+s"(rbi), "+s"(cg), "+s"(c0));
  PINS(x); PINS(w); PINS(wo); PINS(logA); PINS(Aim); PINS(Cre); PINS(Cim); PINS(logdt); PINS(Dsk); PINS(gw); PINS(ws);
  const bool ishelp = l == 6 && (xcd & 1) != (xcd >> 2), iskv = (u32)loc < 16, isssm = loc >= 16;
  u32 tag;
  const int wv = isssm ? wave ^ 4 : wave;
  if (__builtin_expect(!isssm && !ishelp && wave < 4, 1)) {
    tag = role_load(L, x, w, wo, ws, isq, rbi, cg, c0, wave, lane);
  } else if (iskv) {
    tag = role_kv(L, ws, rbi, cg, wave - 4, lane);
  } else if (ishelp) {
    tag = role_helper(L, x, gw, ws, out, Q, wave, lane);
  } else if (isq) {
    tag = role_q(L, ws, rbi, cg, wave - 4, lane);
  } else {
    tag = role_ssm(L, x, logA, Aim, Cre, Cim, logdt, Dsk, ws, xcd * 9 + loc - 16, wave, lane);
  }
  if ((u32)loc < 8) {
    if (blockIdx.x == 56 && wave == 0) {
      const rsrc_t rs = wsrsrc(ws);
      __builtin_amdgcn_s_sleep(32);
#pragma unroll 1
      for (int i = 0; i < 4; ++i) wait_flags<16>(rs, i * 64 + lane < FL_N ? WS_FL + (i * 64 + lane) * FLS : OOR, tag);
      if (lane < 8) st_sc1(rs, WS_EP + lane * 16, u32x4{tag, tag, tag, tag});
    }
    return;
  }
  phase_b(L, ws, out, xcd * 24 + (isq ? l : l - 8), tag, isssm ? FL_BT + xcd * 9 + loc - 16 : 0, iskv, wv, lane);
}

extern "C" __device__ __attribute__((used, noinline)) void gm_code_end() {}

extern "C" void kernel_launch(void* const* d_in, const int* in_sizes, int n_in, void* d_out, int out_size, void* d_ws,
                              size_t ws_size, hipStream_t stream) {
  (void)hipFuncSetAttribute((const void*)gm_k1, hipFuncAttributeMaxDynamicSharedMemorySize, K1_LDS);
  gm_k1<<<256, 512, K1_LDS, stream>>>((const char*)d_in[0], (const char*)d_in[1], (const char*)d_in[2], (const u32*)d_in[4],
                                   (const u32*)d_in[5], (const u32*)d_in[6], (const u32*)d_in[7], (const u16*)d_in[8],
                                   (const u16*)d_in[9], (const char*)d_in[10], (char*)d_ws, (char*)d_out);
}
```
